```python
import torch

def _softmax(x, axis=-1):
    return torch.softmax(x, dim=axis)
def _gelu(x):
    return torch.nn.functional.gelu(x, approximate="tanh")
def _silu(x):
    return torch.nn.functional.silu(x)
def _sigmoid(x):
    return torch.sigmoid(x)
def _layernorm(x, axis=-1, eps=1e-5):
    m = x.mean(dim=axis, keepdim=True)
    v = x.var(dim=axis, keepdim=True, unbiased=False)
    return (x - m) / torch.sqrt(v + eps)
def _scatter_add(tgt, idx, src, axis=0):
    return tgt.clone().index_add_(axis, idx, src)
def _segment_sum(src, seg, n):
    out = torch.zeros((n,) + src.shape[1:], dtype=src.dtype)
    return out.index_add_(0, seg, src)
def _pad(x, pw):
    flat = tuple(v for pair in reversed(pw) for v in pair)
    return torch.nn.functional.pad(x, flat)

kernel_name = 'gated_mix__sliding_block_cached__sliding_block_cached__decode'

def setup_inputs():

    _w = (lambda a: (lambda t: t.to(torch.bfloat16) if t.is_floating_point() else t)(torch.as_tensor(a)))
    def _A_setup():
        B, Sq, D = 4, 1, 512
        Hq, Hkv, Sk, Dh = 32, 8, 8192, 128
        assert Hq % Hkv == 0, (Hq, Hkv)
        return {
            "x": _w((lambda *s: torch.randn(*s, dtype=torch.bfloat16) * 0.05)(B, Sq, D)),
            "k_cache": _w((lambda *s: torch.randn(*s, dtype=torch.bfloat16) * 6.4)(B, Hkv, Sk, Dh)),
            "v_cache": _w((lambda *s: torch.randn(*s, dtype=torch.bfloat16) * 0.05)(B, Hkv, Sk, Dh)),
            "wq": _w((lambda *s: torch.randn(*s, dtype=torch.bfloat16) * 6.4)(D, Hq * Dh) * (D ** -0.5)),
            "wo": _w((lambda *s: torch.randn(*s, dtype=torch.bfloat16) * 0.05)(Hq * Dh, D) * ((Hq * Dh) ** -0.5)),
        }
    _A_in = _A_setup()

    def _B_setup():
        B, Sq, D = 4, 1, 512
        Hq, Hkv, Sk, Dh = 32, 8, 8192, 128
        assert Hq % Hkv == 0, (Hq, Hkv)
        return {
            "x": _w((lambda *s: torch.randn(*s, dtype=torch.bfloat16) * 0.05)(B, Sq, D)),
            "k_cache": _w((lambda *s: torch.randn(*s, dtype=torch.bfloat16) * 6.4)(B, Hkv, Sk, Dh)),
            "v_cache": _w((lambda *s: torch.randn(*s, dtype=torch.bfloat16) * 0.05)(B, Hkv, Sk, Dh)),
            "wq": _w((lambda *s: torch.randn(*s, dtype=torch.bfloat16) * 6.4)(D, Hq * Dh) * (D ** -0.5)),
            "wo": _w((lambda *s: torch.randn(*s, dtype=torch.bfloat16) * 0.05)(Hq * Dh, D) * ((Hq * Dh) ** -0.5)),
        }
    _B_in = _B_setup()

    return {
        "A_x": _A_in["x"],
        "A_k_cache": _A_in["k_cache"],
        "A_v_cache": _A_in["v_cache"],
        "A_wq": _A_in["wq"],
        "A_wo": _A_in["wo"],
        "B_x": _B_in["x"],
        "B_k_cache": _B_in["k_cache"],
        "B_v_cache": _B_in["v_cache"],
        "B_wq": _B_in["wq"],
        "B_wo": _B_in["wo"],
        "gate_w": _w((lambda *s: torch.randn(*s, dtype=torch.bfloat16) * 0.05)(512) * 16.0),
    }

def reference(A_x, A_k_cache, A_v_cache, A_wq, A_wo, B_x, B_k_cache, B_v_cache, B_wq, B_wo, gate_w):
    def _A_ref(x, k_cache, v_cache, wq, wo):
        B, Sq, D = x.shape
        Hq, Dh = 32, 128
        Hkv, Sk = k_cache.shape[1], k_cache.shape[2]
        W = min(4096, max(256, Sk // 4))
        rep = Hq // Hkv
        q = (lambda x, *d: x.permute(*d))((x @ wq).reshape(B, Sq, Hq, Dh), 0, 2, 1, 3)
        k = (lambda x, n, axis: torch.repeat_interleave(x, n, dim=axis))(k_cache[:, :, Sk - W:, :], rep, axis=1)
        v = (lambda x, n, axis: torch.repeat_interleave(x, n, dim=axis))(v_cache[:, :, Sk - W:, :], rep, axis=1)
        scores = torch.einsum("bhqd,bhkd->bhqk", q, k) * (Dh ** -0.5)
        p = _softmax(scores, axis=-1)
        o = torch.einsum("bhqk,bhkd->bhqd", p, v)
        return (lambda x, *d: x.permute(*d))(o, 0, 2, 1, 3).reshape(B, Sq, Hq * Dh) @ wo

    def _B_ref(x, k_cache, v_cache, wq, wo):
        B, Sq, D = x.shape
        Hq, Dh = 32, 128
        Hkv, Sk = k_cache.shape[1], k_cache.shape[2]
        W = min(4096, max(256, Sk // 4))
        rep = Hq // Hkv
        q = (lambda x, *d: x.permute(*d))((x @ wq).reshape(B, Sq, Hq, Dh), 0, 2, 1, 3)
        k = (lambda x, n, axis: torch.repeat_interleave(x, n, dim=axis))(k_cache[:, :, Sk - W:, :], rep, axis=1)
        v = (lambda x, n, axis: torch.repeat_interleave(x, n, dim=axis))(v_cache[:, :, Sk - W:, :], rep, axis=1)
        scores = torch.einsum("bhqd,bhkd->bhqk", q, k) * (Dh ** -0.5)
        p = _softmax(scores, axis=-1)
        o = torch.einsum("bhqk,bhkd->bhqd", p, v)
        return (lambda x, *d: x.permute(*d))(o, 0, 2, 1, 3).reshape(B, Sq, Hq * Dh) @ wo

    x = A_x
    g = _sigmoid((x * gate_w).sum(-1, keepdims=True))
    a = _A_ref(x, A_k_cache, A_v_cache, A_wq, A_wo)
    b = _B_ref(x, B_k_cache, B_v_cache, B_wq, B_wo)
    return g * a + (1 - g) * b
```

```cpp
#include <hip/hip_runtime.h>
#include <hip/hip_bf16.h>
typedef unsigned short u16;
constexpr int NB=4, DM=512, HQ=32, HKV=8, SK=8192, DH=128, W=2048;
__device__ __forceinline__ float b2f(u16 v){ return __uint_as_float(((unsigned)v)<<16); }
__device__ __forceinline__ u16 f2b(float f){ __hip_bfloat16 h=__float2bfloat16(f); return *(u16*)&h; }

__global__ void k_q(const u16* x, const u16* wqA, const u16* wqB, float* q){
  int br=blockIdx.y; int col=blockIdx.x*256+threadIdx.x;
  const u16* wq= br? wqB: wqA;
  float a[4]={0,0,0,0};
  for(int k=0;k<DM;k++){ float w=b2f(wq[(size_t)k*4096+col]);
    for(int b=0;b<4;b++) a[b]+=b2f(x[b*DM+k])*w; }
  for(int b=0;b<4;b++) q[(br*4+b)*4096+col]=b2f(f2b(a[b]));
}
__global__ void k_attn(const float* q, const u16* kA,const u16* vA,const u16* kB,const u16* vB,float* o){
  int h=blockIdx.x, b=blockIdx.y, br=blockIdx.z; int kv=h/4;
  const u16* K=(br?kB:kA)+((size_t)(b*HKV+kv)*SK+(SK-W))*DH;
  const u16* V=(br?vB:vA)+((size_t)(b*HKV+kv)*SK+(SK-W))*DH;
  __shared__ float s[W]; __shared__ float qq[DH]; __shared__ float red[256];
  int t=threadIdx.x;
  if(t<DH) qq[t]=q[(br*4+b)*4096+h*DH+t];
  __syncthreads();
  float mx=-INFINITY;
  for(int k=t;k<W;k+=256){ float a=0; for(int d=0;d<DH;d++) a+=qq[d]*b2f(K[(size_t)k*DH+d]); a*=0.08838834764831845f; s[k]=a; mx=fmaxf(mx,a);}
  red[t]=mx; __syncthreads();
  for(int i=128;i>0;i>>=1){ if(t<i) red[t]=fmaxf(red[t],red[t+i]); __syncthreads(); }
  mx=red[0]; __syncthreads();
  float sm=0; for(int k=t;k<W;k+=256){ float e=__expf(s[k]-mx); s[k]=e; sm+=e; }
  red[t]=sm; __syncthreads();
  for(int i=128;i>0;i>>=1){ if(t<i) red[t]+=red[t+i]; __syncthreads(); }
  sm=red[0]; __syncthreads();
  int d=t&127, half=t>>7; float a=0;
  for(int k=half;k<W;k+=2) a+=s[k]*b2f(V[(size_t)k*DH+d]);
  red[t]=a; __syncthreads();
  if(t<128) o[(br*4+b)*4096+h*DH+t]=b2f(f2b((red[t]+red[t+128])/sm));
}
__global__ void k_o(const float* o,const u16* woA,const u16* woB,float* part){
  int c=blockIdx.x, br=blockIdx.y, n=threadIdx.x; const u16* wo=br?woB:woA;
  float a[4]={0,0,0,0};
  for(int r=c*128;r<c*128+128;r++){ float w=b2f(wo[(size_t)r*512+n]); for(int b=0;b<4;b++) a[b]+=o[(br*4+b)*4096+r]*w; }
  for(int b=0;b<4;b++) part[((br*32+c)*4+b)*512+n]=a[b];
}
__global__ void k_fin(const float* part,const u16* x,const u16* gw,u16* out){
  int b=blockIdx.x,n=threadIdx.x; __shared__ float red[512];
  red[n]=b2f(x[b*DM+n])*b2f(gw[n]); __syncthreads();
  for(int i=256;i>0;i>>=1){ if(n<i) red[n]+=red[n+i]; __syncthreads(); }
  float g=1.f/(1.f+__expf(-red[0]));
  float a=0,bb=0; for(int c=0;c<32;c++){ a+=part[((0*32+c)*4+b)*512+n]; bb+=part[((32+c)*4+b)*512+n]; }
  out[b*512+n]=f2b(g*a+(1.f-g)*bb);
}
extern "C" void kernel_launch(void* const* d_in, const int* in_sizes, int n_in, void* d_out, int out_size, void* d_ws, size_t ws_size, hipStream_t stream){
  const u16* x=(const u16*)d_in[0];
  float* q=(float*)d_ws; float* o=q+2*4*4096; float* part=o+2*4*4096;
  k_q<<<dim3(16,2),256,0,stream>>>(x,(const u16*)d_in[3],(const u16*)d_in[8],q);
  k_attn<<<dim3(32,4,2),256,0,stream>>>(q,(const u16*)d_in[1],(const u16*)d_in[2],(const u16*)d_in[6],(const u16*)d_in[7],o);
  k_o<<<dim3(32,2),512,0,stream>>>(o,(const u16*)d_in[4],(const u16*)d_in[9],part);
  k_fin<<<4,512,0,stream>>>(part,x,(const u16*)d_in[10],(u16*)d_out);
}
```

```cpp
#include <hip/hip_runtime.h>
#include <hip/hip_bf16.h>

typedef unsigned u32;
typedef unsigned long long u64;
typedef u32 v4u __attribute__((ext_vector_type(4)));
typedef u32 v2u __attribute__((ext_vector_type(2)));
typedef float v4f __attribute__((ext_vector_type(4)));
typedef float v2f __attribute__((ext_vector_type(2)));
typedef u64 v2p __attribute__((ext_vector_type(2)));
typedef u64 v4p __attribute__((ext_vector_type(4)));
typedef __bf16 v8b __attribute__((ext_vector_type(8)));
typedef __bf16 v2b __attribute__((ext_vector_type(2)));
#define LAS __attribute__((address_space(3)))

constexpr int TAU = 246;
constexpr int TOFF = -60;
constexpr int GOFF = 10;
constexpr int GWAIT = 300;
constexpr int WIN = 16;
constexpr int PV0 = 21, PV1 = 25, PV2 = 31;
constexpr int ODELAY = 11, FDELAY = 24;
constexpr int CODE_BYTES = 13240;

constexpr int NT = 2, SC1 = 16;
constexpr u32 WS_EPOCH = 0;
constexpr u32 WS_T0 = 256;
constexpr u32 WS_QX = 4096;
constexpr u32 WS_OX = WS_QX + 256 * 2048;
constexpr u32 WS_ML = WS_OX + 256 * 2048;
constexpr u32 WS_FX = WS_ML + 256 * 128;
constexpr u32 WS_END = WS_FX + 256 * 2048;
constexpr int QB = 9;
constexpr int SPIN_CAP = 1 << 15;
constexpr float LOG2E = 1.4426950408889634f, SCALE = 0.08838834764831845f;

constexpr int C_QP = 0, C_QS = 4, C_WO = 8, C_P = 12, C_O = 16, L_TB = 32;
constexpr int L_MLS = 64, L_QS = 256, L_P = L_QS + 1024, L_QP = L_P + 4096;
constexpr int PLANE = 1024 + 128, PART = 2 * PLANE;
constexpr int L_OP = L_QP + 4 * 2048, L_FP = L_OP + 4 * PART, L_END = L_FP + 4 * PART;

struct Args {
  const void* p[8];
  const void *x, *gw;
  void *out, *ws;
};

__device__ __forceinline__ __amdgpu_buffer_rsrc_t mkrsrc(const void* p, u32 bytes) {
  return __builtin_amdgcn_make_buffer_rsrc((void*)p, 0, bytes, 0x00020000);
}
template <int AUX> __device__ __forceinline__ v4u ld16(__amdgpu_buffer_rsrc_t r, int voff, int soff) {
  return __builtin_amdgcn_raw_buffer_load_b128(r, voff, soff, AUX);
}
template <int N> __device__ __forceinline__ void wait_vm() { asm volatile("s_waitcnt vmcnt(%0)" ::"n"(N) : "memory"); }
__device__ __forceinline__ void wg_barrier() {
  __builtin_amdgcn_sched_barrier(0);
  asm volatile("s_waitcnt lgkmcnt(0)\n\ts_barrier" ::: "memory");
  __builtin_amdgcn_sched_barrier(0);
}
template <int CTRL> __device__ __forceinline__ float dpp(float x) {
  return __builtin_bit_cast(float, __builtin_amdgcn_mov_dpp(__builtin_bit_cast(int, x), CTRL, 0xf, 0xf, true));
}
constexpr int XOR1 = 0xB1, XOR2 = 0x4E, XOR7 = 0x141, XOR8 = 0x128;
__device__ __forceinline__ float x16_sum(float x) {
  auto s = __builtin_amdgcn_permlane16_swap(__float_as_uint(x), __float_as_uint(x), false, false);
  return __uint_as_float(s[0]) + __uint_as_float(s[1]);
}
__device__ __forceinline__ float x32_sum(float x) {
  auto s = __builtin_amdgcn_permlane32_swap(__float_as_uint(x), __float_as_uint(x), false, false);
  return __uint_as_float(s[0]) + __uint_as_float(s[1]);
}
__device__ __forceinline__ float x16_max(float x) {
  auto s = __builtin_amdgcn_permlane16_swap(__float_as_uint(x), __float_as_uint(x), false, false);
  return fmaxf(__uint_as_float(s[0]), __uint_as_float(s[1]));
}
__device__ __forceinline__ float x32_max(float x) {
  auto s = __builtin_amdgcn_permlane32_swap(__float_as_uint(x), __float_as_uint(x), false, false);
  return fmaxf(__uint_as_float(s[0]), __uint_as_float(s[1]));
}
__device__ __forceinline__ u32 pkbf(float lo, float hi) {
  v2b r = {(__bf16)lo, (__bf16)hi};
  return __builtin_bit_cast(u32, r);
}
__device__ __forceinline__ float bflo(u32 p) { return __uint_as_float(p << 16); }
__device__ __forceinline__ float bfhi(u32 p) { return __uint_as_float(p & 0xffff0000u); }
__device__ __forceinline__ float rbf(float x) { return bflo(pkbf(x, x)); }
__device__ __forceinline__ v4f mfma(v4u a, v4u b, v4f c) {
  return __builtin_amdgcn_mfma_f32_16x16x32_bf16(__builtin_bit_cast(v8b, a), __builtin_bit_cast(v8b, b), c, 0, 0, 0);
}
__device__ __forceinline__ u32 pair_lo(v4u a, v4u b, int i) { return __builtin_amdgcn_perm(b[i], a[i], 0x05040100u); }
__device__ __forceinline__ u32 pair_hi(v4u a, v4u b, int i) { return __builtin_amdgcn_perm(b[i], a[i], 0x07060302u); }
__device__ __forceinline__ v4u tr_lo(const v4u* t, int i) { return v4u{pair_lo(t[0], t[1], i), pair_lo(t[2], t[3], i), pair_lo(t[4], t[5], i), pair_lo(t[6], t[7], i)}; }
__device__ __forceinline__ v4u tr_hi(const v4u* t, int i) { return v4u{pair_hi(t[0], t[1], i), pair_hi(t[2], t[3], i), pair_hi(t[4], t[5], i), pair_hi(t[6], t[7], i)}; }

template <int TB> __device__ __forceinline__ float val(u32 d) { return __uint_as_float(d & ~((1u << TB) - 1u)); }
__device__ __forceinline__ u32 tagq(float v, u32 t) { return ((__float_as_uint(v) + (1u << (QB - 1))) & ~((1u << QB) - 1u)) | t; }
__device__ __forceinline__ v4u tag12(v4f s, u32 vmask, u32 t) {
  return v4u{(__float_as_uint(s[0]) & vmask) | t, (__float_as_uint(s[1]) & vmask) | t, (__float_as_uint(s[2]) & vmask) | t, (__float_as_uint(s[3]) & vmask) | t};
}
__device__ __forceinline__ u32 differs(v4u g, u32 t) {
  return __builtin_amdgcn_bitop3_b32(g[0], g[1], t, 0x7E) | __builtin_amdgcn_bitop3_b32(g[2], g[3], t, 0x7E);
}
__device__ __forceinline__ void settle2(v4u& a, v4u& b, __amdgpu_buffer_rsrc_t r, int voff0, int voff1, int soff, u32 t12) {
  u32 n;
  asm volatile(
      "s_movk_i32 %2, 0x7fff\n"
      "1:\n\t"
      "v_bitop3_b32 v184, v176, v177, %6 bitop3:0x7e\n\t"
      "v_bitop3_b32 v185, v178, v179, %6 bitop3:0x7e\n\t"
      "v_bitop3_b32 v186, v180, v181, %6 bitop3:0x7e\n\t"
      "v_bitop3_b32 v187, v182, v183, %6 bitop3:0x7e\n\t"
      "v_or3_b32 v184, v184, v185, v186\n\t"
      "v_bitop3_b32 v184, v184, %7, v187 bitop3:0xc8\n\t"
      "v_cmp_ne_u32 vcc, 0, v184\n\t"
      "s_cbranch_vccz 2f\n\t"
      "s_and_b64 exec, exec, vcc\n\t"
      "s_sub_u32 %2, %2, 1\n\t"
      "s_cbranch_scc1 2f\n\t"
      "buffer_load_dwordx4 v[176:179], %3, %4, %5 offen sc1\n\t"
      "buffer_load_dwordx4 v[180:183], %8, %4, %5 offen sc1\n\t"
      "s_waitcnt vmcnt(0)\n\t"
      "s_branch 1b\n"
      "2:\n\t"
      "s_mov_b64 exec, -1"
      : "+{v[176:179]}"(a), "+{v[180:183]}"(b), "=&s"(n)
      : "v"(voff0), "s"(r), "s"(soff), "s"(t12), "s"(0xfffu), "v"(voff1)
      : "v184", "v185", "v186", "v187", "vcc", "scc", "memory");
}

template <int OFF> __device__ __forceinline__ void lds_arrive(u32 zero, u32 one) {
  asm volatile("s_mov_b64 exec, 1\n\tds_add_u32 %0, %1 offset:%2\n\ts_mov_b64 exec, -1" ::"v"(zero), "v"(one), "n"(OFF) : "memory");
}
template <int SLEEP> __device__ __forceinline__ void lds_wait(LAS unsigned char* p, u32 n) {
  while (__hip_atomic_load((LAS u32*)p, __ATOMIC_RELAXED, __HIP_MEMORY_SCOPE_WORKGROUP) < n) {
    if (SLEEP) __builtin_amdgcn_s_sleep(SLEEP);
  }
  asm volatile("" ::: "memory");
}
template <int OFF, int STRIDE> __device__ __forceinline__ v4f lds_wait_sum4(LAS unsigned char* lds, int o) {
  v4f a, b, c, d;
  u32 n;
  do {
    n = __hip_atomic_load((LAS u32*)(lds + OFF), __ATOMIC_RELAXED, __HIP_MEMORY_SCOPE_WORKGROUP);
    asm volatile("" ::: "memory");
    a = *(LAS v4f*)(lds + o); b = *(LAS v4f*)(lds + o + STRIDE); c = *(LAS v4f*)(lds + o + 2 * STRIDE); d = *(LAS v4f*)(lds + o + 3 * STRIDE);
    asm volatile("" : "+v"(a), "+v"(b), "+v"(c), "+v"(d)::"memory");
  } while (__builtin_amdgcn_ballot_w64(n < 4u) != 0ull);
  return (a + b) + (c + d);
}

template <int TICKS> __device__ __forceinline__ void hold(u32 t0) {
  u32 d;
  asm volatile("1:\n\ts_memrealtime vcc\n\ts_waitcnt lgkmcnt(0)\n\ts_sub_u32 %0, vcc_lo, %1\n\ts_cmpk_lt_i32 %0, %2\n\ts_cbranch_scc1 1b"
               : "=&s"(d) : "s"(t0), "n"(TICKS) : "memory", "scc", "vcc");
}
template <int STEP> __device__ __forceinline__ void pace(u32 t0) { hold<TOFF + STEP * TAU / 16>(t0); }

template <int I> struct IC { static constexpr int value = I; };
template <int B, int E, class F> __device__ __forceinline__ void static_for(F&& f) {
  if constexpr (B < E) { f(IC<B>{}); static_for<B + 1, E>(f); }
}

__global__ void __launch_bounds__(512, 2) gated_mix_decode_kernel(Args a) {
  __shared__ __attribute__((aligned(16))) unsigned char lds_raw[L_END];
  LAS unsigned char* lds = (LAS unsigned char*)lds_raw;
  const int bid = blockIdx.x, lane = threadIdx.x & 63, wave = __builtin_amdgcn_readfirstlane(threadIdx.x >> 6);
  u64 pc;
  v2p pa2;
  v4p pb4;
  asm volatile("s_getpc_b64 %2\n\ts_load_dwordx4 %0, %3, %4\n\ts_load_dwordx8 %1, %3, 0x40"
               : "=&s"(pa2), "=&s"(pb4), "=&s"(pc) : "s"(__builtin_amdgcn_kernarg_segment_ptr()), "s"((bid & 4) * 8 + (wave >> 2) * 16));
  __builtin_amdgcn_sched_barrier(0);
  const int grp = (bid & 7) * 2 + (bid >> 7), m = (bid >> 3) & 15, wg = grp * 16 + m;
  const int kvh = grp & 7, mh = m >> 2, ml = m & 3;
  const int w = wave & 3, g = lane >> 4, c = lane & 15;
  const bool vrole = wave < 4;
  const int kvbase = ((mh * 8 + kvh) * 8192 + 6144 + ml * 512) * 256, rot = (bid * 37 + w) & 127;
  const int koff = ((lane >> 2) & 3) * 256 + g * 64 + (lane & 3) * 16;
  const u32 pfc = __builtin_amdgcn_raw_buffer_load_b32(mkrsrc((const void*)(pc & ~255ull), vrole ? CODE_BYTES : 0), c * 64 + g * 65536, (ml * 4 + w) * 1024, 0);
  int vo = vrole ? g * 8 * 8192 + c * 16 : koff, n0 = vrole ? 512 * 8192 : 4 * 8 * 8192 * 256, nx = vrole ? 4096 : 0;
  const int sb = vrole ? (mh * 128 + 32 * w) * 8192 + kvh * 1024 + ml * 256 : kvbase, sr = vrole ? 0 : rot * 1024, st = vrole ? 8192 : 4096;
  int xo = ((lane >> 2) & 3) * 1024 + g * 16, xs = (mh * 128 + 32 * w) * 2, so[8];
#pragma unroll
  for (int i = 0; i < 8; ++i) so[i] = sb + ((sr + i * st) & 0x1ffff);
  u64 t64;
  asm volatile("s_waitcnt lgkmcnt(0)\n\ts_memrealtime %0"
               : "=&s"(t64), "+s"(pa2), "+s"(pb4), "+v"(vo), "+v"(xo), "+s"(n0), "+s"(nx), "+s"(xs), "+s"(so[0]), "+s"(so[1]), "+s"(so[2]),
                 "+s"(so[3]), "+s"(so[4]), "+s"(so[5]), "+s"(so[6]), "+s"(so[7]));
  const void *p0 = (const void*)pa2[0], *p1 = (const void*)pa2[1], *px = (const void*)pb4[0], *pg = (const void*)pb4[1];
  const void *po = (const void*)pb4[2], *pw = (const void*)pb4[3];
  const __amdgpu_buffer_rsrc_t rws = mkrsrc(pw, WS_END);
  const v4u xa = ld16<0>(mkrsrc(px, nx), xo, xs);
  v4u t[32];
  {
    const __amdgpu_buffer_rsrc_t r0 = mkrsrc(p0, n0);
#pragma unroll
    for (int i = 0; i < 8; ++i) t[i] = ld16<NT>(r0, vo, so[i]);
  }
  __builtin_amdgcn_sched_barrier(0);
  u32 ep;
  asm volatile("s_load_dword %0, %1, 0x0" : "=&s"(ep) : "s"(pw) : "memory");
  if (threadIdx.x < 8) *(LAS u32*)(lds + threadIdx.x * 4) = 0u;
  u32 vzero = 0u, vone = 1u, vmask = ~0xfffu;
  asm volatile("" : "+v"(vzero), "+v"(vone), "+v"(vmask));
  wg_barrier();
  asm volatile("" : "+s"(ep), "+s"(t64));
  u32 t0 = (u32)t64;
  const u32 tag = ep + 1u, t12 = tag & 0xfffu;

  if (wave < 4) {
    const __amdgpu_buffer_rsrc_t rv = mkrsrc(p1, 4 * 8 * 8192 * 256);
    const int fb = bid >> 6, fc = bid & 63;
    const u32 ng = w == 2 ? 1024u : 0u;
    const v4u xv = ld16<0>(mkrsrc((const char*)px + fb * 1024, ng), lane * 16, 0), gv = ld16<0>(mkrsrc(pg, ng), lane * 16, 0);
    __builtin_amdgcn_sched_barrier(0);
    {
      float qp[8];
#pragma unroll
      for (int i = 0; i < 4; ++i) {
        qp[2 * i] = mfma(xa, tr_lo(t, i), v4f{0, 0, 0, 0})[0];
        qp[2 * i + 1] = mfma(xa, tr_hi(t, i), v4f{0, 0, 0, 0})[0];
      }
      *(LAS v4f*)(lds + L_QP + w * 2048 + lane * 32) = v4f{qp[0], qp[1], qp[2], qp[3]};
      *(LAS v4f*)(lds + L_QP + w * 2048 + lane * 32 + 16) = v4f{qp[4], qp[5], qp[6], qp[7]};
      lds_arrive<C_QP>(vzero, vone);
      if (w < 2) {
        lds_wait<0>(lds + C_QP, 4u);
        const int o = L_QP + w * 1024 + lane * 16;
        const v4f s = (*(LAS v4f*)(lds + o) + *(LAS v4f*)(lds + o + 2048)) + (*(LAS v4f*)(lds + o + 4096) + *(LAS v4f*)(lds + o + 6144));
        const u32 tq = tag & ((1u << QB) - 1u);
        __builtin_amdgcn_raw_buffer_store_b128(v4u{tagq(s[0], tq), tagq(s[1], tq), tagq(s[2], tq), tagq(s[3], tq)}, rws, lane * 16, WS_QX + wg * 2048 + w * 1024, SC1);
      }
    }
    float gate = 0.f;
    if (w == 2) {
      float z = 0.f;
#pragma unroll
      for (int i = 0; i < 4; ++i) { const u32 p = pkbf(bflo(xv[i]) * bflo(gv[i]), bfhi(xv[i]) * bfhi(gv[i])); z += bflo(p) + bfhi(p); }
      z += dpp<XOR1>(z); z += dpp<XOR2>(z); z += dpp<XOR7>(z); z += dpp<XOR8>(z);
      z = rbf(x32_sum(x16_sum(z)));
      gate = rbf(1.f / (1.f + __expf(-z)));
      asm volatile("" : "+v"(gate));
    }
    u32 pf0 = 0, pf1 = 0;
    if (w == 3) {
      hold<GWAIT>(t0);
      const v2u e = __builtin_amdgcn_raw_buffer_load_b64(rws, (lane & 7) * 8, WS_T0, SC1);
      pf0 = __builtin_amdgcn_raw_buffer_load_b32(rws, (lane & 31) * 64 + (lane >> 5) * (WS_FX - WS_OX), WS_OX + wg * 2048, 0);
      pf1 = __builtin_amdgcn_raw_buffer_load_b32(rws, 0, WS_ML + wg * 128, 0);
      int dt = e[1] == tag ? (int)(e[0] - t0) : 0;
      dt = dt < -300 ? -300 : dt > 300 ? 300 : dt;
      dt += __builtin_amdgcn_mov_dpp(dt, XOR1, 0xf, 0xf, true);
      dt += __builtin_amdgcn_mov_dpp(dt, XOR2, 0xf, 0xf, true);
      dt += __builtin_amdgcn_mov_dpp(dt, XOR7, 0xf, 0xf, true);
      *(LAS u32*)(lds + L_TB) = t0 + (u32)((dt >> 3) + GOFF);
    }
    wg_barrier();
    t0 = __builtin_amdgcn_readfirstlane(*(LAS u32*)(lds + L_TB));
    v4u pa[4];
    v4f acc[8];
    float eo;
    int ow = L_OP + w * PART + g * 576 + (c & 3) * 144 + (c >> 2) * 4, orr = L_OP + (2 * w + (lane >> 5)) * 576 + ((lane >> 3) & 3) * 144 + (lane & 7) * 16;
    int src = ((lane >> 2) * 16 + (lane & 3) * 4) * 2048;
    int opub = __builtin_amdgcn_readfirstlane(WS_OX + wg * 2048 + w * 1024);
    int fsrc = __builtin_amdgcn_readfirstlane(WS_FX + (fc >> 4) * 2048 + fb * 512 + (fc & 15) * 32), odst = __builtin_amdgcn_readfirstlane((fb * 512 + fc * 8) * 2);
    float gate1 = rbf(1.f - gate);
    asm volatile("" : "+s"(opub), "+s"(fsrc), "+s"(odst), "+v"(gate1));
    const __amdgpu_buffer_rsrc_t rout = mkrsrc(po, 4096);
    static_for<0, 32>([&](auto ic) {
      constexpr int i = ic.value;
      if (i >= WIN) wait_vm<WIN - 1>();
      pace<42 + i>(t0);
      t[i] = ld16<NT>(rv, lane * 16, kvbase + ((4 * i + rot) & 127) * 1024);
      if (i == 31) __hip_atomic_store((LAS u32*)(lds + C_WO), 1u, __ATOMIC_RELAXED, __HIP_MEMORY_SCOPE_WORKGROUP);
      if (i == PV0 || i == PV1 || i == PV2) {
        constexpr int b = i == PV0 ? 0 : i == PV1 ? 1 : 2;
        __builtin_amdgcn_sched_barrier(0);
        if (b == 0) {
          lds_wait<0>(lds + C_P, 4u);
          float mw[4];
#pragma unroll
          for (int j = 0; j < 4; ++j) mw[j] = *(LAS float*)(lds + L_MLS + ((w + j) & 3) * 32 + g * 8);
#pragma unroll
          for (int j = 0; j < 4; ++j) pa[j] = *(LAS v4u*)(lds + L_P + w * 1024 + j * 256 + (lane & ~3) * 4);
          eo = __builtin_amdgcn_exp2f((mw[0] - fmaxf(fmaxf(mw[0], mw[1]), fmaxf(mw[2], mw[3]))) * LOG2E);
          asm volatile("" : "+v"(eo), "+v"(pa[1]), "+v"(pa[2]), "+v"(pa[3]), "+v"(ow), "+v"(orr), "+v"(src));
        }
#pragma unroll
        for (int j = 0; j < 4; ++j) {
          acc[2 * j] = mfma(pa[b], tr_lo(t + 8 * b, j), b ? acc[2 * j] : v4f{0, 0, 0, 0});
          acc[2 * j + 1] = mfma(pa[b], tr_hi(t + 8 * b, j), b ? acc[2 * j + 1] : v4f{0, 0, 0, 0});
        }
        __builtin_amdgcn_sched_barrier(0);
      }
    });
    asm volatile("" ::"v"(pf0), "v"(pf1), "v"(pfc));
    {
      v4u vt[8];
#pragma unroll
      for (int r = 0; r < 4; ++r) {
#pragma unroll
        for (int i = 0; i < 4; ++i) {
          vt[2 * i][r] = pair_lo(t[24 + 2 * r], t[25 + 2 * r], i);
          vt[2 * i + 1][r] = pair_hi(t[24 + 2 * r], t[25 + 2 * r], i);
        }
        if (r < 3) __builtin_amdgcn_sched_barrier(0);
      }
#pragma unroll
      for (int j = 0; j < 8; ++j) acc[j] = mfma(pa[3], vt[j], acc[j]);
    }
#pragma unroll
    for (int j = 0; j < 8; ++j) *(LAS float*)(lds + ow + j * 16) = acc[j][0] * eo;
    lds_arrive<C_O>(vzero, vone);
    if (w < 2) __builtin_amdgcn_raw_buffer_store_b128(tag12(lds_wait_sum4<C_O, PART>(lds, orr), vmask, t12), rws, lane * 16, opub, SC1);
    wg_barrier();
    if (w == 2) {
      asm volatile(".p2align 6");
      __builtin_amdgcn_s_sleep(FDELAY);
      v4u f0 = ld16<SC1>(rws, src, fsrc), f1 = ld16<SC1>(rws, src + 16, fsrc);
      settle2(f0, f1, rws, src, src + 16, fsrc, t12);
      float y[4];
#pragma unroll
      for (int i = 0; i < 4; ++i) {
        const float lo = val<12>(f0[i]), hi = val<12>(f1[i]), a0 = lo + dpp<XOR8>(lo), a1 = hi + dpp<XOR8>(hi);
        y[i] = (lane & 8) ? a1 : a0;
      }
#pragma unroll
      for (int i = 0; i < 2; ++i) {
        const float a0 = y[i] + dpp<XOR7>(y[i]), a1 = y[i + 2] + dpp<XOR7>(y[i + 2]);
        y[i] = (lane & 4) ? a1 : a0;
      }
      const float a0 = y[0] + dpp<XOR2>(y[0]), a1 = y[1] + dpp<XOR2>(y[1]);
      float s = (lane & 2) ? a1 : a0;
      s += dpp<XOR1>(s);
      s = x16_sum(s);
      const auto ab = __builtin_amdgcn_permlane32_swap(__float_as_uint(s), __float_as_uint(s), false, false);
      const u32 p = pkbf(__uint_as_float(ab[0]), __uint_as_float(ab[1]));
      const u32 r = pkbf(gate * bflo(p), gate1 * bfhi(p));
      const u32 y16 = pkbf(bflo(r) + bfhi(r), 0.f);
      if (lane < 16 && !(lane & 1)) __builtin_amdgcn_raw_buffer_store_b16((unsigned short)y16, rout, lane & 14, odst, SC1);
      if (bid == 0 && lane == 0) __builtin_amdgcn_raw_buffer_store_b32(tag, rws, 0, WS_EPOCH, SC1);
    }
  } else {
    const __amdgpu_buffer_rsrc_t rk = mkrsrc(p0, 4 * 8 * 8192 * 256), ro = mkrsrc(p1, 4096 * 1024);
    if (bid < 8 && w == 0 && lane == 0) __builtin_amdgcn_raw_buffer_store_b64(v2u{t0, tag}, rws, 0, WS_T0 + bid * 8, SC1);
    static_for<8, 32>([&](auto ic) {
      constexpr int i = ic.value;
      if (i >= WIN) wait_vm<WIN - 1>();
      pace<8 + i>(t0);
      t[i] = ld16<NT>(rk, koff, kvbase + ((4 * i + rot) & 127) * 1024);
    });
    const int qoff = (lane >> 5) * 8 * 2048 + (lane & 31) * 16, qsrc = WS_QX + (grp * 16 + w) * 2048 + mh * 512;
    v4u gq0 = ld16<SC1>(rws, qoff, qsrc), gq1 = ld16<SC1>(rws, qoff, qsrc + 4 * 2048);
    wg_barrier();
    t0 = __builtin_amdgcn_readfirstlane(*(LAS u32*)(lds + L_TB));
    {
      bool ok = ((differs(gq0, tag) | differs(gq1, tag)) & ((1u << QB) - 1u)) == 0u;
#pragma unroll 1
      for (int it = 0; it < SPIN_CAP && !__all(ok); ++it) {
        if (!ok) {
          gq0 = ld16<SC1>(rws, qoff, qsrc);
          gq1 = ld16<SC1>(rws, qoff, qsrc + 4 * 2048);
          ok = ((differs(gq0, tag) | differs(gq1, tag)) & ((1u << QB) - 1u)) == 0u;
        }
        asm volatile("" ::: "memory");
      }
      float qv[4];
#pragma unroll
      for (int i = 0; i < 4; ++i) qv[i] = x32_sum(val<QB>(gq0[i]) + val<QB>(gq1[i]));
      if (lane < 32) *(LAS v2u*)(lds + L_QS + w * 256 + lane * 8) = v2u{pkbf(qv[0], qv[1]), pkbf(qv[2], qv[3])};
      lds_arrive<C_QS>(vzero, vone);
      lds_wait<0>(lds + C_QS, 4u);
    }
    const v4u qb = *(LAS v4u*)(lds + L_QS + koff);
    const int q = lane & 3;
    float sc[8];
#pragma unroll
    for (int b = 0; b < 4; ++b) {
      float raw[2];
#pragma unroll
      for (int u = 0; u < 2; ++u) {
        const v4f d0 = mfma(t[8 * b + u], qb, v4f{0, 0, 0, 0}), d1 = mfma(t[8 * b + 2 + u], qb, v4f{0, 0, 0, 0});
        const v4f d2 = mfma(t[8 * b + 4 + u], qb, v4f{0, 0, 0, 0}), d3 = mfma(t[8 * b + 6 + u], qb, v4f{0, 0, 0, 0});
        const float r0 = dpp<0xFF>(d0[3]) + (dpp<0xAA>(d0[2]) + (dpp<0x55>(d0[1]) + d0[0]));
        const float r1 = dpp<0xFF>(d1[3]) + (dpp<0xAA>(d1[2]) + (dpp<0x00>(d1[0]) + d1[1]));
        const float r2 = dpp<0xFF>(d2[3]) + (dpp<0x55>(d2[1]) + (dpp<0x00>(d2[0]) + d2[2]));
        const float r3 = dpp<0xAA>(d3[2]) + (dpp<0x55>(d3[1]) + (dpp<0x00>(d3[0]) + d3[3]));
        const float s0 = (q & 1) ? r1 : r0, s1 = (q & 1) ? r3 : r2;
        raw[u] = (q & 2) ? s1 : s0;
      }
      const u32 p1 = pkbf(raw[0], raw[1]);
      const u32 p2 = pkbf(bflo(p1) * SCALE, bfhi(p1) * SCALE);
      sc[2 * b] = bflo(p2);
      sc[2 * b + 1] = bfhi(p2);
    }
    float mx = fmaxf(fmaxf(fmaxf(sc[0], sc[1]), fmaxf(sc[2], sc[3])), fmaxf(fmaxf(sc[4], sc[5]), fmaxf(sc[6], sc[7])));
    mx = fmaxf(mx, dpp<XOR1>(mx));
    mx = fmaxf(mx, dpp<XOR2>(mx));
    mx = x32_max(x16_max(mx));
    float ls = 0.f;
#pragma unroll
    for (int b = 0; b < 4; ++b) {
      const float e0 = __builtin_amdgcn_exp2f((sc[2 * b] - mx) * LOG2E), e1 = __builtin_amdgcn_exp2f((sc[2 * b + 1] - mx) * LOG2E);
      ls += e0 + e1;
      *(LAS u32*)(lds + L_P + w * 1024 + b * 256 + lane * 4) = pkbf(e0, e1);
    }
    ls += dpp<XOR1>(ls);
    ls += dpp<XOR2>(ls);
    ls = x32_sum(x16_sum(ls));
    if (lane < 16 && q == 0) *(LAS v2f*)(lds + L_MLS + w * 32 + (lane >> 2) * 8) = v2f{mx, ls};
    lds_arrive<C_P>(vzero, vone);
    if (w == 3) {
      lds_wait<1>(lds + C_P, 4u);
      float mw[4], lw[4];
#pragma unroll
      for (int i = 0; i < 4; ++i) { const v2f x = *(LAS v2f*)(lds + L_MLS + i * 32 + g * 8); mw[i] = x[0]; lw[i] = x[1]; }
      const float mg = fmaxf(fmaxf(mw[0], mw[1]), fmaxf(mw[2], mw[3]));
      float lg = 0.f;
#pragma unroll
      for (int i = 0; i < 4; ++i) lg += __builtin_amdgcn_exp2f((mw[i] - mg) * LOG2E) * lw[i];
      const u32 um = __float_as_uint(mg) | t12, ul = (__float_as_uint(lg) & vmask) | t12;
      if (c == 0) __builtin_amdgcn_raw_buffer_store_b128(v4u{um, ul, um, ul}, rws, g * 16, WS_ML + wg * 128, SC1);
    }
    lds_wait<1>(lds + C_WO, 1u);
    const int xa0 = 8 * w + g + 4 * (mh & 1);
    v4u gm = ld16<SC1>(rws, c * 128, WS_ML + grp * 16 * 128 + mh * 16);
    const int hb = (kvh * 4 + mh) * 128 * 1024 + ml * 256;
    int fw = L_FP + w * PART + lane * 16, fr = L_FP + (lane & 1) * PLANE + (w * 32 + (lane >> 1)) * 16, oo0 = c * 2048 + xa0 * 16, oo1 = oo0 ^ 64;
    int os = WS_OX + grp * 16 * 2048 + mh * 512, fpub = WS_FX + wg * 2048 + w * 1024;
    asm volatile("" : "+v"(fw), "+v"(fr), "+v"(oo0), "+v"(oo1), "+s"(os), "+s"(fpub));
    static_for<0, 8>([&](auto ic) {
      constexpr int i = ic.value;
      pace<74 + i>(t0);
      t[i] = ld16<NT>(ro, (i < 4 ? xa0 : xa0 ^ 4) * 1024 + c * 16, hb + 32 * (i & 3) * 1024);
    });
    lds_wait<1>(lds + C_O, 4u);
    asm volatile(".p2align 6");
    __builtin_amdgcn_s_sleep(ODELAY);
    v4u o0 = ld16<SC1>(rws, oo0, os), o1 = ld16<SC1>(rws, oo1, os);
    __builtin_amdgcn_sched_barrier(0);
    float cf;
    {
      bool ok = (differs(gm, t12) & 0xfffu) == 0u;
#pragma unroll 1
      for (int it = 0; it < SPIN_CAP && !__all(ok); ++it) {
        if (!ok) {
          gm = ld16<SC1>(rws, c * 128, WS_ML + grp * 16 * 128 + mh * 16);
          ok = (differs(gm, t12) & 0xfffu) == 0u;
        }
        asm volatile("" ::: "memory");
      }
      const float ms = val<12>(gm[0]);
      float M = fmaxf(ms, dpp<XOR1>(ms));
      M = fmaxf(M, dpp<XOR2>(M));
      const float e = __builtin_amdgcn_exp2f((ms - M) * LOG2E);
      float L = e * val<12>(gm[1]);
      L += dpp<XOR1>(L);
      L += dpp<XOR2>(L);
      cf = e * __builtin_amdgcn_rcpf(L);
    }
    asm volatile("" : "+v"(cf));
    __builtin_amdgcn_sched_barrier(0);
    v4u wt[8];
#pragma unroll
    for (int r = 0; r < 4; ++r)
#pragma unroll
      for (int i = 0; i < 4; ++i) {
        u32 lo = pair_lo(t[2 * r], t[2 * r + 1], i), hi = pair_hi(t[2 * r], t[2 * r + 1], i);
        asm volatile("" : "+v"(lo), "+v"(hi));
        wt[2 * i][r] = lo;
        wt[2 * i + 1][r] = hi;
      }
    __builtin_amdgcn_sched_barrier(0);
    settle2(o0, o1, rws, oo0, oo1, os, t12);
    float ov[8];
#pragma unroll
    for (int i = 0; i < 4; ++i) { ov[i] = cf * val<12>(o0[i]); ov[4 + i] = cf * val<12>(o1[i]); }
#define QADD(i, p) "v_add_f32_dpp %" #i ", %" #i ", %" #i " quad_perm:" p " row_mask:0xf bank_mask:0xf\n\t"
#define QADD8(p) QADD(0, p) QADD(1, p) QADD(2, p) QADD(3, p) QADD(4, p) QADD(5, p) QADD(6, p) QADD(7, p)
    asm volatile("s_nop 1\n\t" QADD8("[1,0,3,2]") QADD8("[2,3,0,1]")
                 : "+v"(ov[0]), "+v"(ov[1]), "+v"(ov[2]), "+v"(ov[3]), "+v"(ov[4]), "+v"(ov[5]), "+v"(ov[6]), "+v"(ov[7]));
    const v4u oa = {pkbf(ov[0], ov[1]), pkbf(ov[2], ov[3]), pkbf(ov[4], ov[5]), pkbf(ov[6], ov[7])};
    float f[8];
#pragma unroll
    for (int j = 0; j < 8; ++j) f[j] = mfma(oa, wt[j], v4f{0, 0, 0, 0})[0];
    *(LAS v4f*)(lds + fw) = v4f{f[0], f[1], f[2], f[3]};
    *(LAS v4f*)(lds + fw + PLANE) = v4f{f[4], f[5], f[6], f[7]};
    wg_barrier();
    if (w < 2) {
      const v4f s = (*(LAS v4f*)(lds + fr) + *(LAS v4f*)(lds + fr + PART)) + (*(LAS v4f*)(lds + fr + 2 * PART) + *(LAS v4f*)(lds + fr + 3 * PART));
      __builtin_amdgcn_raw_buffer_store_b128(tag12(s, vmask, t12), rws, lane * 16, fpub, SC1);
    }
  }
}

extern "C" void kernel_launch(void* const* d_in, const int* in_sizes, int n_in, void* d_out, int out_size, void* d_ws, size_t ws_size,
                              hipStream_t stream) {
  Args a;
  a.p[0] = d_in[3]; a.p[1] = d_in[2]; a.p[2] = d_in[1]; a.p[3] = d_in[4];
  a.p[4] = d_in[8]; a.p[5] = d_in[7]; a.p[6] = d_in[6]; a.p[7] = d_in[9];
  a.x = d_in[0];
  a.gw = d_in[10];
  a.out = d_out;
  a.ws = d_ws;
  gated_mix_decode_kernel<<<256, 512, 0, stream>>>(a);
}
```

```cpp
#include <hip/hip_runtime.h>
#include <hip/hip_bf16.h>

typedef unsigned u32;
typedef unsigned long long u64;
typedef u32 v4u __attribute__((ext_vector_type(4)));
typedef u32 v2u __attribute__((ext_vector_type(2)));
typedef float v4f __attribute__((ext_vector_type(4)));
typedef float v2f __attribute__((ext_vector_type(2)));
typedef u64 v2p __attribute__((ext_vector_type(2)));
typedef u64 v4p __attribute__((ext_vector_type(4)));
typedef __bf16 v8b __attribute__((ext_vector_type(8)));
typedef __bf16 v2b __attribute__((ext_vector_type(2)));
#define LAS __attribute__((address_space(3)))

constexpr int TAU = 246;
constexpr int TOFF = -60;
constexpr int GOFF = 10;
constexpr int GWAIT = 300;
constexpr int WIN = 16;
constexpr int PV0 = 21, PV1 = 25, PV2 = 31;
constexpr int ODELAY = 11, FDELAY = 24;
constexpr int CODE_BYTES = 13304;

constexpr int NT = 2, SC1 = 16;
constexpr u32 WS_EPOCH = 0;
constexpr u32 WS_T0 = 256;
constexpr u32 WS_QX = 4096;
constexpr u32 WS_OX = WS_QX + 256 * 2048;
constexpr u32 WS_ML = WS_OX + 256 * 2048;
constexpr u32 WS_FX = WS_ML + 256 * 128;
constexpr u32 WS_END = WS_FX + 256 * 2048;
constexpr int QB = 9;
constexpr int SPIN_CAP = 1 << 15;
constexpr float LOG2E = 1.4426950408889634f, SCALE = 0.08838834764831845f;

constexpr int C_QP = 0, C_QS = 4, C_WO = 8, C_P = 12, C_O = 16, L_TB = 32;
constexpr int L_MLS = 64, L_QS = 256, L_P = L_QS + 1024, L_QP = L_P + 4096;
constexpr int PLANE = 1024 + 128, PART = 2 * PLANE;
constexpr int L_OP = L_QP + 4 * 2048, L_FP = L_OP + 4 * PART, L_END = L_FP + 4 * PART;

struct Args {
  const void* p[8];
  const void *x, *gw;
  void *out, *ws;
};

__device__ __forceinline__ __amdgpu_buffer_rsrc_t mkrsrc(const void* p, u32 bytes) {
  return __builtin_amdgcn_make_buffer_rsrc((void*)p, 0, bytes, 0x00020000);
}
template <int AUX> __device__ __forceinline__ v4u ld16(__amdgpu_buffer_rsrc_t r, int voff, int soff) {
  return __builtin_amdgcn_raw_buffer_load_b128(r, voff, soff, AUX);
}
template <int N> __device__ __forceinline__ void wait_vm() { asm volatile("s_waitcnt vmcnt(%0)" ::"n"(N) : "memory"); }
__device__ __forceinline__ void wg_barrier() {
  __builtin_amdgcn_sched_barrier(0);
  asm volatile("s_waitcnt lgkmcnt(0)\n\ts_barrier" ::: "memory");
  __builtin_amdgcn_sched_barrier(0);
}
template <int CTRL> __device__ __forceinline__ float dpp(float x) {
  return __builtin_bit_cast(float, __builtin_amdgcn_mov_dpp(__builtin_bit_cast(int, x), CTRL, 0xf, 0xf, true));
}
constexpr int XOR1 = 0xB1, XOR2 = 0x4E, XOR7 = 0x141, XOR8 = 0x128;
__device__ __forceinline__ float x16_sum(float x) {
  auto s = __builtin_amdgcn_permlane16_swap(__float_as_uint(x), __float_as_uint(x), false, false);
  return __uint_as_float(s[0]) + __uint_as_float(s[1]);
}
__device__ __forceinline__ float x32_sum(float x) {
  auto s = __builtin_amdgcn_permlane32_swap(__float_as_uint(x), __float_as_uint(x), false, false);
  return __uint_as_float(s[0]) + __uint_as_float(s[1]);
}
__device__ __forceinline__ float x16_max(float x) {
  auto s = __builtin_amdgcn_permlane16_swap(__float_as_uint(x), __float_as_uint(x), false, false);
  return fmaxf(__uint_as_float(s[0]), __uint_as_float(s[1]));
}
__device__ __forceinline__ float x32_max(float x) {
  auto s = __builtin_amdgcn_permlane32_swap(__float_as_uint(x), __float_as_uint(x), false, false);
  return fmaxf(__uint_as_float(s[0]), __uint_as_float(s[1]));
}
__device__ __forceinline__ u32 pkbf(float lo, float hi) {
  v2b r = {(__bf16)lo, (__bf16)hi};
  return __builtin_bit_cast(u32, r);
}
__device__ __forceinline__ float bflo(u32 p) { return __uint_as_float(p << 16); }
__device__ __forceinline__ float bfhi(u32 p) { return __uint_as_float(p & 0xffff0000u); }
__device__ __forceinline__ float rbf(float x) { return bflo(pkbf(x, x)); }
__device__ __forceinline__ v4f mfma(v4u a, v4u b, v4f c) {
  return __builtin_amdgcn_mfma_f32_16x16x32_bf16(__builtin_bit_cast(v8b, a), __builtin_bit_cast(v8b, b), c, 0, 0, 0);
}
__device__ __forceinline__ u32 pair_lo(v4u a, v4u b, int i) { return __builtin_amdgcn_perm(b[i], a[i], 0x05040100u); }
__device__ __forceinline__ u32 pair_hi(v4u a, v4u b, int i) { return __builtin_amdgcn_perm(b[i], a[i], 0x07060302u); }
__device__ __forceinline__ v4u tr_lo(const v4u* t, int i) { return v4u{pair_lo(t[0], t[1], i), pair_lo(t[2], t[3], i), pair_lo(t[4], t[5], i), pair_lo(t[6], t[7], i)}; }
__device__ __forceinline__ v4u tr_hi(const v4u* t, int i) { return v4u{pair_hi(t[0], t[1], i), pair_hi(t[2], t[3], i), pair_hi(t[4], t[5], i), pair_hi(t[6], t[7], i)}; }

template <int TB> __device__ __forceinline__ float val(u32 d) { return __uint_as_float(d & ~((1u << TB) - 1u)); }
__device__ __forceinline__ u32 tagq(float v, u32 t) { return ((__float_as_uint(v) + (1u << (QB - 1))) & ~((1u << QB) - 1u)) | t; }
__device__ __forceinline__ v4u tag12(v4f s, u32 vmask, u32 t) {
  return v4u{(__float_as_uint(s[0]) & vmask) | t, (__float_as_uint(s[1]) & vmask) | t, (__float_as_uint(s[2]) & vmask) | t, (__float_as_uint(s[3]) & vmask) | t};
}
__device__ __forceinline__ u32 differs(v4u g, u32 t) {
  return __builtin_amdgcn_bitop3_b32(g[0], g[1], t, 0x7E) | __builtin_amdgcn_bitop3_b32(g[2], g[3], t, 0x7E);
}
__device__ __forceinline__ void settle2(v4u& a, v4u& b, __amdgpu_buffer_rsrc_t r, int voff0, int voff1, int soff, u32 t12) {
  u32 n;
  asm volatile(
      "s_movk_i32 %2, 0x7fff\n"
      "1:\n\t"
      "v_bitop3_b32 v184, v176, v177, %6 bitop3:0x7e\n\t"
      "v_bitop3_b32 v185, v178, v179, %6 bitop3:0x7e\n\t"
      "v_bitop3_b32 v186, v180, v181, %6 bitop3:0x7e\n\t"
      "v_bitop3_b32 v187, v182, v183, %6 bitop3:0x7e\n\t"
      "v_or3_b32 v184, v184, v185, v186\n\t"
      "v_bitop3_b32 v184, v184, %7, v187 bitop3:0xc8\n\t"
      "v_cmp_ne_u32 vcc, 0, v184\n\t"
      "s_cbranch_vccz 2f\n\t"
      "s_and_b64 exec, exec, vcc\n\t"
      "s_sub_u32 %2, %2, 1\n\t"
      "s_cbranch_scc1 2f\n\t"
      "buffer_load_dwordx4 v[176:179], %3, %4, %5 offen sc1\n\t"
      "buffer_load_dwordx4 v[180:183], %8, %4, %5 offen sc1\n\t"
      "s_waitcnt vmcnt(0)\n\t"
      "s_branch 1b\n"
      "2:\n\t"
      "s_mov_b64 exec, -1"
      : "+{v[176:179]}"(a), "+{v[180:183]}"(b), "=&s"(n)
      : "v"(voff0), "s"(r), "s"(soff), "s"(t12), "s"(0xfffu), "v"(voff1)
      : "v184", "v185", "v186", "v187", "vcc", "scc", "memory");
}

template <int OFF> __device__ __forceinline__ void lds_arrive(u32 zero, u32 one) {
  asm volatile("s_mov_b64 exec, 1\n\tds_add_u32 %0, %1 offset:%2\n\ts_mov_b64 exec, -1" ::"v"(zero), "v"(one), "n"(OFF) : "memory");
}
template <int SLEEP> __device__ __forceinline__ void lds_wait(LAS unsigned char* p, u32 n) {
  while (__hip_atomic_load((LAS u32*)p, __ATOMIC_RELAXED, __HIP_MEMORY_SCOPE_WORKGROUP) < n) {
    if (SLEEP) __builtin_amdgcn_s_sleep(SLEEP);
  }
  asm volatile("" ::: "memory");
}
template <int OFF, int STRIDE> __device__ __forceinline__ v4f lds_wait_sum4(LAS unsigned char* lds, int o) {
  v4f a, b, c, d;
  u32 n;
  do {
    n = __hip_atomic_load((LAS u32*)(lds + OFF), __ATOMIC_RELAXED, __HIP_MEMORY_SCOPE_WORKGROUP);
    asm volatile("" ::: "memory");
    a = *(LAS v4f*)(lds + o); b = *(LAS v4f*)(lds + o + STRIDE); c = *(LAS v4f*)(lds + o + 2 * STRIDE); d = *(LAS v4f*)(lds + o + 3 * STRIDE);
    asm volatile("" : "+v"(a), "+v"(b), "+v"(c), "+v"(d)::"memory");
  } while (__builtin_amdgcn_ballot_w64(n < 4u) != 0ull);
  return (a + b) + (c + d);
}

template <int TICKS> __device__ __forceinline__ void hold(u32 t0) {
  u32 d;
  asm volatile("1:\n\ts_memrealtime vcc\n\ts_waitcnt lgkmcnt(0)\n\ts_sub_u32 %0, vcc_lo, %1\n\ts_cmpk_lt_i32 %0, %2\n\ts_cbranch_scc1 1b"
               : "=&s"(d) : "s"(t0), "n"(TICKS) : "memory", "scc", "vcc");
}
template <int STEP> __device__ __forceinline__ void pace(u32 t0) { hold<TOFF + STEP * TAU / 16>(t0); }

template <int I> struct IC { static constexpr int value = I; };
template <int B, int E, class F> __device__ __forceinline__ void static_for(F&& f) {
  if constexpr (B < E) { f(IC<B>{}); static_for<B + 1, E>(f); }
}

__global__ void __launch_bounds__(512, 2) gated_mix_decode_kernel(Args a) {
  __shared__ __attribute__((aligned(16))) unsigned char lds_raw[L_END];
  LAS unsigned char* lds = (LAS unsigned char*)lds_raw;
  const int bid = blockIdx.x, lane = threadIdx.x & 63, wave = __builtin_amdgcn_readfirstlane(threadIdx.x >> 6);
  u64 pc;
  v2p pa2;
  v4p pb4;
  asm volatile("s_getpc_b64 %2\n\ts_load_dwordx4 %0, %3, %4\n\ts_load_dwordx8 %1, %3, 0x40"
               : "=&s"(pa2), "=&s"(pb4), "=&s"(pc) : "s"(__builtin_amdgcn_kernarg_segment_ptr()), "s"((bid & 4) * 8 + (wave >> 2) * 16));
  __builtin_amdgcn_sched_barrier(0);
  const int grp = (bid & 7) * 2 + (bid >> 7), m = (bid >> 3) & 15, wg = grp * 16 + m;
  const int kvh = grp & 7, mh = m >> 2, ml = m & 3;
  const int w = wave & 3, g = lane >> 4, c = lane & 15;
  const bool vrole = wave < 4;
  const int kvbase = ((mh * 8 + kvh) * 8192 + 6144 + ml * 512) * 256, rot = (bid * 37 + w) & 127;
  const int koff = ((lane >> 2) & 3) * 256 + g * 64 + (lane & 3) * 16;
  const u32 pfc = __builtin_amdgcn_raw_buffer_load_b32(mkrsrc((const void*)(pc & ~255ull), vrole ? CODE_BYTES : 0), c * 64 + g * 65536, (ml * 4 + w) * 1024, 0);
  int vo = vrole ? g * 8 * 8192 + c * 16 : koff, n0 = vrole ? 512 * 8192 : 4 * 8 * 8192 * 256, nx = vrole ? 4096 : 0;
  const int sb = vrole ? (mh * 128 + 32 * w) * 8192 + kvh * 1024 + ml * 256 : kvbase, sr = vrole ? 0 : rot * 1024, st = vrole ? 8192 : 4096;
  int xo = ((lane >> 2) & 3) * 1024 + g * 16, xs = (mh * 128 + 32 * w) * 2, so[8];
#pragma unroll
  for (int i = 0; i < 8; ++i) so[i] = sb + ((sr + i * st) & 0x1ffff);
  u64 t64;
  asm volatile("s_waitcnt lgkmcnt(0)\n\ts_memrealtime %0"
               : "=&s"(t64), "+s"(pa2), "+s"(pb4), "+v"(vo), "+v"(xo), "+s"(n0), "+s"(nx), "+s"(xs), "+s"(so[0]), "+s"(so[1]), "+s"(so[2]),
                 "+s"(so[3]), "+s"(so[4]), "+s"(so[5]), "+s"(so[6]), "+s"(so[7]));
  const void *p0 = (const void*)pa2[0], *p1 = (const void*)pa2[1], *px = (const void*)pb4[0], *pg = (const void*)pb4[1];
  const void *po = (const void*)pb4[2], *pw = (const void*)pb4[3];
  const __amdgpu_buffer_rsrc_t rws = mkrsrc(pw, WS_END);
  const v4u xa = ld16<0>(mkrsrc(px, nx), xo, xs);
  v4u t[32];
  {
    const __amdgpu_buffer_rsrc_t r0 = mkrsrc(p0, n0);
#pragma unroll
    for (int i = 0; i < 8; ++i) t[i] = ld16<NT>(r0, vo, so[i]);
  }
  __builtin_amdgcn_sched_barrier(0);
  u32 ep;
  asm volatile("s_load_dword %0, %1, 0x0" : "=&s"(ep) : "s"(pw) : "memory");
  if (threadIdx.x < 8) *(LAS u32*)(lds + threadIdx.x * 4) = 0u;
  u32 vzero = 0u, vone = 1u, vmask = ~0xfffu;
  asm volatile("" : "+v"(vzero), "+v"(vone), "+v"(vmask));
  wg_barrier();
  asm volatile("" : "+s"(ep), "+s"(t64));
  u32 t0 = (u32)t64;
  const u32 tag = ep + 1u, t12 = tag & 0xfffu;

  if (wave < 4) {
    const __amdgpu_buffer_rsrc_t rv = mkrsrc(p1, 4 * 8 * 8192 * 256);
    const int fb = (bid & 7) >> 1, fc = (bid & 1) * 32 + (bid >> 3);
    const u32 ng = w == 2 ? 1024u : 0u;
    const v4u xv = ld16<0>(mkrsrc((const char*)px + fb * 1024, ng), lane * 16, 0), gv = ld16<0>(mkrsrc(pg, ng), lane * 16, 0);
    __builtin_amdgcn_sched_barrier(0);
    {
      float qp[8];
#pragma unroll
      for (int i = 0; i < 4; ++i) {
        qp[2 * i] = mfma(xa, tr_lo(t, i), v4f{0, 0, 0, 0})[0];
        qp[2 * i + 1] = mfma(xa, tr_hi(t, i), v4f{0, 0, 0, 0})[0];
      }
      *(LAS v4f*)(lds + L_QP + w * 2048 + lane * 32) = v4f{qp[0], qp[1], qp[2], qp[3]};
      *(LAS v4f*)(lds + L_QP + w * 2048 + lane * 32 + 16) = v4f{qp[4], qp[5], qp[6], qp[7]};
      lds_arrive<C_QP>(vzero, vone);
      if (w < 2) {
        lds_wait<0>(lds + C_QP, 4u);
        const int o = L_QP + w * 1024 + lane * 16;
        const v4f s = (*(LAS v4f*)(lds + o) + *(LAS v4f*)(lds + o + 2048)) + (*(LAS v4f*)(lds + o + 4096) + *(LAS v4f*)(lds + o + 6144));
        const u32 tq = tag & ((1u << QB) - 1u);
        __builtin_amdgcn_raw_buffer_store_b128(v4u{tagq(s[0], tq), tagq(s[1], tq), tagq(s[2], tq), tagq(s[3], tq)}, rws, lane * 16, WS_QX + wg * 2048 + w * 1024, SC1);
      }
    }
    float gate = 0.f;
    if (w == 2) {
      float z = 0.f;
#pragma unroll
      for (int i = 0; i < 4; ++i) { const u32 p = pkbf(bflo(xv[i]) * bflo(gv[i]), bfhi(xv[i]) * bfhi(gv[i])); z += bflo(p) + bfhi(p); }
      z += dpp<XOR1>(z); z += dpp<XOR2>(z); z += dpp<XOR7>(z); z += dpp<XOR8>(z);
      z = rbf(x32_sum(x16_sum(z)));
      gate = rbf(1.f / (1.f + __expf(-z)));
      asm volatile("" : "+v"(gate));
    }
    u32 pf0 = 0, pf1 = 0, pf2 = 0;
    if (w == 3) {
      hold<GWAIT>(t0);
      const v2u e = __builtin_amdgcn_raw_buffer_load_b64(rws, (lane & 7) * 8, WS_T0, SC1);
      pf0 = __builtin_amdgcn_raw_buffer_load_b32(rws, (lane & 31) * 64 + (lane >> 5) * (WS_FX - WS_OX), WS_OX + wg * 2048, 0);
      pf1 = __builtin_amdgcn_raw_buffer_load_b32(rws, 0, WS_ML + wg * 128, 0);
      pf2 = __builtin_amdgcn_raw_buffer_load_b32(mkrsrc(po, 4096), 0, (fb * 512 + fc * 8) * 2, 0);
      int dt = e[1] == tag ? (int)(e[0] - t0) : 0;
      dt = dt < -300 ? -300 : dt > 300 ? 300 : dt;
      dt += __builtin_amdgcn_mov_dpp(dt, XOR1, 0xf, 0xf, true);
      dt += __builtin_amdgcn_mov_dpp(dt, XOR2, 0xf, 0xf, true);
      dt += __builtin_amdgcn_mov_dpp(dt, XOR7, 0xf, 0xf, true);
      *(LAS u32*)(lds + L_TB) = t0 + (u32)((dt >> 3) + GOFF);
    }
    wg_barrier();
    t0 = __builtin_amdgcn_readfirstlane(*(LAS u32*)(lds + L_TB));
    v4u pa[4];
    v4f acc[8];
    float eo;
    int ow = L_OP + w * PART + g * 576 + (c & 3) * 144 + (c >> 2) * 4, orr = L_OP + (2 * w + (lane >> 5)) * 576 + ((lane >> 3) & 3) * 144 + (lane & 7) * 16;
    int src = ((lane >> 2) * 16 + (lane & 3) * 4) * 2048;
    int opub = __builtin_amdgcn_readfirstlane(WS_OX + wg * 2048 + w * 1024);
    int fsrc = __builtin_amdgcn_readfirstlane(WS_FX + (fc >> 4) * 2048 + fb * 512 + (fc & 15) * 32), odst = __builtin_amdgcn_readfirstlane((fb * 512 + fc * 8) * 2);
    float gate1 = rbf(1.f - gate);
    asm volatile("" : "+s"(opub), "+s"(fsrc), "+s"(odst), "+v"(gate1));
    const __amdgpu_buffer_rsrc_t rout = mkrsrc(po, 4096);
    static_for<0, 32>([&](auto ic) {
      constexpr int i = ic.value;
      if (i >= WIN) wait_vm<WIN - 1>();
      pace<42 + i>(t0);
      t[i] = ld16<NT>(rv, lane * 16, kvbase + ((4 * i + rot) & 127) * 1024);
      if (i == 31) __hip_atomic_store((LAS u32*)(lds + C_WO), 1u, __ATOMIC_RELAXED, __HIP_MEMORY_SCOPE_WORKGROUP);
      if (i == PV0 || i == PV1 || i == PV2) {
        constexpr int b = i == PV0 ? 0 : i == PV1 ? 1 : 2;
        __builtin_amdgcn_sched_barrier(0);
        if (b == 0) {
          lds_wait<0>(lds + C_P, 4u);
          float mw[4];
#pragma unroll
          for (int j = 0; j < 4; ++j) mw[j] = *(LAS float*)(lds + L_MLS + ((w + j) & 3) * 32 + g * 8);
#pragma unroll
          for (int j = 0; j < 4; ++j) pa[j] = *(LAS v4u*)(lds + L_P + w * 1024 + j * 256 + (lane & ~3) * 4);
          eo = __builtin_amdgcn_exp2f((mw[0] - fmaxf(fmaxf(mw[0], mw[1]), fmaxf(mw[2], mw[3]))) * LOG2E);
          asm volatile("" : "+v"(eo), "+v"(pa[1]), "+v"(pa[2]), "+v"(pa[3]), "+v"(ow), "+v"(orr), "+v"(src));
        }
#pragma unroll
        for (int j = 0; j < 4; ++j) {
          acc[2 * j] = mfma(pa[b], tr_lo(t + 8 * b, j), b ? acc[2 * j] : v4f{0, 0, 0, 0});
          acc[2 * j + 1] = mfma(pa[b], tr_hi(t + 8 * b, j), b ? acc[2 * j + 1] : v4f{0, 0, 0, 0});
        }
        __builtin_amdgcn_sched_barrier(0);
      }
    });
    asm volatile("" ::"v"(pf0), "v"(pf1), "v"(pf2), "v"(pfc));
    {
      v4u vt[8];
#pragma unroll
      for (int r = 0; r < 4; ++r) {
#pragma unroll
        for (int i = 0; i < 4; ++i) {
          vt[2 * i][r] = pair_lo(t[24 + 2 * r], t[25 + 2 * r], i);
          vt[2 * i + 1][r] = pair_hi(t[24 + 2 * r], t[25 + 2 * r], i);
        }
        if (r < 3) __builtin_amdgcn_sched_barrier(0);
      }
#pragma unroll
      for (int j = 0; j < 8; ++j) acc[j] = mfma(pa[3], vt[j], acc[j]);
    }
#pragma unroll
    for (int j = 0; j < 8; ++j) *(LAS float*)(lds + ow + j * 16) = acc[j][0] * eo;
    lds_arrive<C_O>(vzero, vone);
    if (w < 2) __builtin_amdgcn_raw_buffer_store_b128(tag12(lds_wait_sum4<C_O, PART>(lds, orr), vmask, t12), rws, lane * 16, opub, SC1);
    wg_barrier();
    if (w == 2) {
      asm volatile(".p2align 6");
      __builtin_amdgcn_s_sleep(FDELAY);
      v4u f0 = ld16<SC1>(rws, src, fsrc), f1 = ld16<SC1>(rws, src + 16, fsrc);
      settle2(f0, f1, rws, src, src + 16, fsrc, t12);
      float y[4];
#pragma unroll
      for (int i = 0; i < 4; ++i) {
        const float lo = val<12>(f0[i]), hi = val<12>(f1[i]), a0 = lo + dpp<XOR8>(lo), a1 = hi + dpp<XOR8>(hi);
        y[i] = (lane & 8) ? a1 : a0;
      }
#pragma unroll
      for (int i = 0; i < 2; ++i) {
        const float a0 = y[i] + dpp<XOR7>(y[i]), a1 = y[i + 2] + dpp<XOR7>(y[i + 2]);
        y[i] = (lane & 4) ? a1 : a0;
      }
      const float a0 = y[0] + dpp<XOR2>(y[0]), a1 = y[1] + dpp<XOR2>(y[1]);
      float s = (lane & 2) ? a1 : a0;
      s += dpp<XOR1>(s);
      s = x16_sum(s);
      const auto ab = __builtin_amdgcn_permlane32_swap(__float_as_uint(s), __float_as_uint(s), false, false);
      const u32 p = pkbf(__uint_as_float(ab[0]), __uint_as_float(ab[1]));
      const u32 r = pkbf(gate * bflo(p), gate1 * bfhi(p));
      const u32 y16 = pkbf(bflo(r) + bfhi(r), 0.f);
      if (lane < 16 && !(lane & 1)) __builtin_amdgcn_raw_buffer_store_b16((unsigned short)y16, rout, lane & 14, odst, SC1);
      if (bid == 0 && lane == 0) __builtin_amdgcn_raw_buffer_store_b32(tag, rws, 0, WS_EPOCH, SC1);
    }
  } else {
    const __amdgpu_buffer_rsrc_t rk = mkrsrc(p0, 4 * 8 * 8192 * 256), ro = mkrsrc(p1, 4096 * 1024);
    if (bid < 8 && w == 0 && lane == 0) __builtin_amdgcn_raw_buffer_store_b64(v2u{t0, tag}, rws, 0, WS_T0 + bid * 8, SC1);
    static_for<8, 32>([&](auto ic) {
      constexpr int i = ic.value;
      if (i >= WIN) wait_vm<WIN - 1>();
      pace<8 + i>(t0);
      t[i] = ld16<NT>(rk, koff, kvbase + ((4 * i + rot) & 127) * 1024);
    });
    const int qoff = (lane >> 5) * 8 * 2048 + (lane & 31) * 16, qsrc = WS_QX + (grp * 16 + w) * 2048 + mh * 512;
    v4u gq0 = ld16<SC1>(rws, qoff, qsrc), gq1 = ld16<SC1>(rws, qoff, qsrc + 4 * 2048);
    wg_barrier();
    t0 = __builtin_amdgcn_readfirstlane(*(LAS u32*)(lds + L_TB));
    {
      bool ok = ((differs(gq0, tag) | differs(gq1, tag)) & ((1u << QB) - 1u)) == 0u;
#pragma unroll 1
      for (int it = 0; it < SPIN_CAP && !__all(ok); ++it) {
        if (!ok) {
          gq0 = ld16<SC1>(rws, qoff, qsrc);
          gq1 = ld16<SC1>(rws, qoff, qsrc + 4 * 2048);
          ok = ((differs(gq0, tag) | differs(gq1, tag)) & ((1u << QB) - 1u)) == 0u;
        }
        asm volatile("" ::: "memory");
      }
      float qv[4];
#pragma unroll
      for (int i = 0; i < 4; ++i) qv[i] = x32_sum(val<QB>(gq0[i]) + val<QB>(gq1[i]));
      if (lane < 32) *(LAS v2u*)(lds + L_QS + w * 256 + lane * 8) = v2u{pkbf(qv[0], qv[1]), pkbf(qv[2], qv[3])};
      lds_arrive<C_QS>(vzero, vone);
      lds_wait<0>(lds + C_QS, 4u);
    }
    const v4u qb = *(LAS v4u*)(lds + L_QS + koff);
    const int q = lane & 3;
    float sc[8];
#pragma unroll
    for (int b = 0; b < 4; ++b) {
      float raw[2];
#pragma unroll
      for (int u = 0; u < 2; ++u) {
        const v4f d0 = mfma(t[8 * b + u], qb, v4f{0, 0, 0, 0}), d1 = mfma(t[8 * b + 2 + u], qb, v4f{0, 0, 0, 0});
        const v4f d2 = mfma(t[8 * b + 4 + u], qb, v4f{0, 0, 0, 0}), d3 = mfma(t[8 * b + 6 + u], qb, v4f{0, 0, 0, 0});
        const float r0 = dpp<0xFF>(d0[3]) + (dpp<0xAA>(d0[2]) + (dpp<0x55>(d0[1]) + d0[0]));
        const float r1 = dpp<0xFF>(d1[3]) + (dpp<0xAA>(d1[2]) + (dpp<0x00>(d1[0]) + d1[1]));
        const float r2 = dpp<0xFF>(d2[3]) + (dpp<0x55>(d2[1]) + (dpp<0x00>(d2[0]) + d2[2]));
        const float r3 = dpp<0xAA>(d3[2]) + (dpp<0x55>(d3[1]) + (dpp<0x00>(d3[0]) + d3[3]));
        const float s0 = (q & 1) ? r1 : r0, s1 = (q & 1) ? r3 : r2;
        raw[u] = (q & 2) ? s1 : s0;
      }
      const u32 p1 = pkbf(raw[0], raw[1]);
      const u32 p2 = pkbf(bflo(p1) * SCALE, bfhi(p1) * SCALE);
      sc[2 * b] = bflo(p2);
      sc[2 * b + 1] = bfhi(p2);
    }
    float mx = fmaxf(fmaxf(fmaxf(sc[0], sc[1]), fmaxf(sc[2], sc[3])), fmaxf(fmaxf(sc[4], sc[5]), fmaxf(sc[6], sc[7])));
    mx = fmaxf(mx, dpp<XOR1>(mx));
    mx = fmaxf(mx, dpp<XOR2>(mx));
    mx = x32_max(x16_max(mx));
    float ls = 0.f;
#pragma unroll
    for (int b = 0; b < 4; ++b) {
      const float e0 = __builtin_amdgcn_exp2f((sc[2 * b] - mx) * LOG2E), e1 = __builtin_amdgcn_exp2f((sc[2 * b + 1] - mx) * LOG2E);
      ls += e0 + e1;
      *(LAS u32*)(lds + L_P + w * 1024 + b * 256 + lane * 4) = pkbf(e0, e1);
    }
    ls += dpp<XOR1>(ls);
    ls += dpp<XOR2>(ls);
    ls = x32_sum(x16_sum(ls));
    if (lane < 16 && q == 0) *(LAS v2f*)(lds + L_MLS + w * 32 + (lane >> 2) * 8) = v2f{mx, ls};
    lds_arrive<C_P>(vzero, vone);
    if (w == 3) {
      lds_wait<1>(lds + C_P, 4u);
      float mw[4], lw[4];
#pragma unroll
      for (int i = 0; i < 4; ++i) { const v2f x = *(LAS v2f*)(lds + L_MLS + i * 32 + g * 8); mw[i] = x[0]; lw[i] = x[1]; }
      const float mg = fmaxf(fmaxf(mw[0], mw[1]), fmaxf(mw[2], mw[3]));
      float lg = 0.f;
#pragma unroll
      for (int i = 0; i < 4; ++i) lg += __builtin_amdgcn_exp2f((mw[i] - mg) * LOG2E) * lw[i];
      const u32 um = __float_as_uint(mg) | t12, ul = (__float_as_uint(lg) & vmask) | t12;
      if (c == 0) __builtin_amdgcn_raw_buffer_store_b128(v4u{um, ul, um, ul}, rws, g * 16, WS_ML + wg * 128, SC1);
    }
    lds_wait<1>(lds + C_WO, 1u);
    const int xa0 = 8 * w + g + 4 * (mh & 1);
    v4u gm = ld16<SC1>(rws, c * 128, WS_ML + grp * 16 * 128 + mh * 16);
    const int hb = (kvh * 4 + mh) * 128 * 1024 + ml * 256;
    int fw = L_FP + w * PART + lane * 16, fr = L_FP + (lane & 1) * PLANE + (w * 32 + (lane >> 1)) * 16, oo0 = c * 2048 + xa0 * 16, oo1 = oo0 ^ 64;
    int os = WS_OX + grp * 16 * 2048 + mh * 512, fpub = WS_FX + wg * 2048 + w * 1024;
    asm volatile("" : "+v"(fw), "+v"(fr), "+v"(oo0), "+v"(oo1), "+s"(os), "+s"(fpub));
    static_for<0, 8>([&](auto ic) {
      constexpr int i = ic.value;
      pace<74 + i>(t0);
      t[i] = ld16<NT>(ro, (i < 4 ? xa0 : xa0 ^ 4) * 1024 + c * 16, hb + 32 * (i & 3) * 1024);
    });
    lds_wait<1>(lds + C_O, 4u);
    asm volatile(".p2align 6");
    __builtin_amdgcn_s_sleep(ODELAY);
    v4u o0 = ld16<SC1>(rws, oo0, os), o1 = ld16<SC1>(rws, oo1, os);
    __builtin_amdgcn_sched_barrier(0);
    float cf;
    {
      bool ok = (differs(gm, t12) & 0xfffu) == 0u;
#pragma unroll 1
      for (int it = 0; it < SPIN_CAP && !__all(ok); ++it) {
        if (!ok) {
          gm = ld16<SC1>(rws, c * 128, WS_ML + grp * 16 * 128 + mh * 16);
          ok = (differs(gm, t12) & 0xfffu) == 0u;
        }
        asm volatile("" ::: "memory");
      }
      const float ms = val<12>(gm[0]);
      float M = fmaxf(ms, dpp<XOR1>(ms));
      M = fmaxf(M, dpp<XOR2>(M));
      const float e = __builtin_amdgcn_exp2f((ms - M) * LOG2E);
      float L = e * val<12>(gm[1]);
      L += dpp<XOR1>(L);
      L += dpp<XOR2>(L);
      cf = e * __builtin_amdgcn_rcpf(L);
    }
    asm volatile("" : "+v"(cf));
    __builtin_amdgcn_sched_barrier(0);
    v4u wt[8];
#pragma unroll
    for (int r = 0; r < 4; ++r)
#pragma unroll
      for (int i = 0; i < 4; ++i) {
        u32 lo = pair_lo(t[2 * r], t[2 * r + 1], i), hi = pair_hi(t[2 * r], t[2 * r + 1], i);
        asm volatile("" : "+v"(lo), "+v"(hi));
        wt[2 * i][r] = lo;
        wt[2 * i + 1][r] = hi;
      }
    __builtin_amdgcn_sched_barrier(0);
    settle2(o0, o1, rws, oo0, oo1, os, t12);
    float ov[8];
#pragma unroll
    for (int i = 0; i < 4; ++i) { ov[i] = cf * val<12>(o0[i]); ov[4 + i] = cf * val<12>(o1[i]); }
#define QADD(i, p) "v_add_f32_dpp %" #i ", %" #i ", %" #i " quad_perm:" p " row_mask:0xf bank_mask:0xf\n\t"
#define QADD8(p) QADD(0, p) QADD(1, p) QADD(2, p) QADD(3, p) QADD(4, p) QADD(5, p) QADD(6, p) QADD(7, p)
    asm volatile("s_nop 1\n\t" QADD8("[1,0,3,2]") QADD8("[2,3,0,1]")
                 : "+v"(ov[0]), "+v"(ov[1]), "+v"(ov[2]), "+v"(ov[3]), "+v"(ov[4]), "+v"(ov[5]), "+v"(ov[6]), "+v"(ov[7]));
    const v4u oa = {pkbf(ov[0], ov[1]), pkbf(ov[2], ov[3]), pkbf(ov[4], ov[5]), pkbf(ov[6], ov[7])};
    float f[8];
#pragma unroll
    for (int j = 0; j < 8; ++j) f[j] = mfma(oa, wt[j], v4f{0, 0, 0, 0})[0];
    *(LAS v4f*)(lds + fw) = v4f{f[0], f[1], f[2], f[3]};
    *(LAS v4f*)(lds + fw + PLANE) = v4f{f[4], f[5], f[6], f[7]};
    wg_barrier();
    if (w < 2) {
      const v4f s = (*(LAS v4f*)(lds + fr) + *(LAS v4f*)(lds + fr + PART)) + (*(LAS v4f*)(lds + fr + 2 * PART) + *(LAS v4f*)(lds + fr + 3 * PART));
      __builtin_amdgcn_raw_buffer_store_b128(tag12(s, vmask, t12), rws, lane * 16, fpub, SC1);
    }
  }
}

extern "C" void kernel_launch(void* const* d_in, const int* in_sizes, int n_in, void* d_out, int out_size, void* d_ws, size_t ws_size,
                              hipStream_t stream) {
  Args a;
  a.p[0] = d_in[3]; a.p[1] = d_in[2]; a.p[2] = d_in[1]; a.p[3] = d_in[4];
  a.p[4] = d_in[8]; a.p[5] = d_in[7]; a.p[6] = d_in[6]; a.p[7] = d_in[9];
  a.x = d_in[0];
  a.gw = d_in[10];
  a.out = d_out;
  a.ws = d_ws;
  gated_mix_decode_kernel<<<256, 512, 0, stream>>>(a);
}
```

```cpp
#include <hip/hip_runtime.h>
#include <hip/hip_bf16.h>

typedef unsigned u32;
typedef unsigned long long u64;
typedef u32 v4u __attribute__((ext_vector_type(4)));
typedef u32 v2u __attribute__((ext_vector_type(2)));
typedef float v4f __attribute__((ext_vector_type(4)));
typedef float v2f __attribute__((ext_vector_type(2)));
typedef u64 v2p __attribute__((ext_vector_type(2)));
typedef u64 v4p __attribute__((ext_vector_type(4)));
typedef __bf16 v8b __attribute__((ext_vector_type(8)));
typedef __bf16 v2b __attribute__((ext_vector_type(2)));
#define LAS __attribute__((address_space(3)))

constexpr int TAU = 246;
constexpr int TOFF = -60;
constexpr int GOFF = 10;
constexpr int GWAIT = 300;
constexpr int WIN = 16;
constexpr int PV0 = 21, PV1 = 25, PV2 = 31;
constexpr int ODELAY = 11, FDELAY = 24;
constexpr int CODE_BYTES = 13592;

constexpr int NT = 2, SC1 = 16;
constexpr u32 WS_EPOCH = 0;
constexpr u32 WS_T0 = 256;
constexpr u32 WS_QX = 4096;
constexpr u32 WS_OX = WS_QX + 256 * 2048;
constexpr u32 WS_ML = WS_OX + 256 * 2048;
constexpr u32 WS_FX = WS_ML + 256 * 128;
constexpr u32 WS_END = WS_FX + 256 * 2048;
constexpr int QB = 9;
constexpr int SPIN_CAP = 1 << 15;
constexpr float LOG2E = 1.4426950408889634f, SCALE = 0.08838834764831845f;

constexpr int C_QP = 0, C_QS = 4, C_WO = 8, C_P = 12, C_O = 16, L_TB = 32;
constexpr int L_MLS = 64, L_QS = 256, L_P = L_QS + 1024, L_QP = L_P + 4096;
constexpr int PLANE = 1024 + 128, PART = 2 * PLANE;
constexpr int L_OP = L_QP + 4 * 2048, L_FP = L_OP + 4 * PART, L_END = L_FP + 4 * PART;

struct Args {
  const void* p[8];
  const void *x, *gw;
  void *out, *ws;
};

__device__ __forceinline__ __amdgpu_buffer_rsrc_t mkrsrc(const void* p, u32 bytes) {
  return __builtin_amdgcn_make_buffer_rsrc((void*)p, 0, bytes, 0x00020000);
}
template <int AUX> __device__ __forceinline__ v4u ld16(__amdgpu_buffer_rsrc_t r, int voff, int soff) {
  return __builtin_amdgcn_raw_buffer_load_b128(r, voff, soff, AUX);
}
template <int N> __device__ __forceinline__ void wait_vm() { asm volatile("s_waitcnt vmcnt(%0)" ::"n"(N) : "memory"); }
__device__ __forceinline__ void wg_barrier() {
  __builtin_amdgcn_sched_barrier(0);
  asm volatile("s_waitcnt lgkmcnt(0)\n\ts_barrier" ::: "memory");
  __builtin_amdgcn_sched_barrier(0);
}
template <int CTRL> __device__ __forceinline__ float dpp(float x) {
  return __builtin_bit_cast(float, __builtin_amdgcn_mov_dpp(__builtin_bit_cast(int, x), CTRL, 0xf, 0xf, true));
}
constexpr int XOR1 = 0xB1, XOR2 = 0x4E, XOR7 = 0x141, XOR8 = 0x128;
__device__ __forceinline__ float x16_sum(float x) {
  auto s = __builtin_amdgcn_permlane16_swap(__float_as_uint(x), __float_as_uint(x), false, false);
  return __uint_as_float(s[0]) + __uint_as_float(s[1]);
}
__device__ __forceinline__ float x32_sum(float x) {
  auto s = __builtin_amdgcn_permlane32_swap(__float_as_uint(x), __float_as_uint(x), false, false);
  return __uint_as_float(s[0]) + __uint_as_float(s[1]);
}
__device__ __forceinline__ float x16_max(float x) {
  auto s = __builtin_amdgcn_permlane16_swap(__float_as_uint(x), __float_as_uint(x), false, false);
  return fmaxf(__uint_as_float(s[0]), __uint_as_float(s[1]));
}
__device__ __forceinline__ float x32_max(float x) {
  auto s = __builtin_amdgcn_permlane32_swap(__float_as_uint(x), __float_as_uint(x), false, false);
  return fmaxf(__uint_as_float(s[0]), __uint_as_float(s[1]));
}
__device__ __forceinline__ u32 pkbf(float lo, float hi) {
  v2b r = {(__bf16)lo, (__bf16)hi};
  return __builtin_bit_cast(u32, r);
}
__device__ __forceinline__ float bflo(u32 p) { return __uint_as_float(p << 16); }
__device__ __forceinline__ float bfhi(u32 p) { return __uint_as_float(p & 0xffff0000u); }
__device__ __forceinline__ float rbf(float x) { return bflo(pkbf(x, x)); }
__device__ __forceinline__ v4f mfma(v4u a, v4u b, v4f c) {
  return __builtin_amdgcn_mfma_f32_16x16x32_bf16(__builtin_bit_cast(v8b, a), __builtin_bit_cast(v8b, b), c, 0, 0, 0);
}
__device__ __forceinline__ u32 pair_lo(v4u a, v4u b, int i) { return __builtin_amdgcn_perm(b[i], a[i], 0x05040100u); }
__device__ __forceinline__ u32 pair_hi(v4u a, v4u b, int i) { return __builtin_amdgcn_perm(b[i], a[i], 0x07060302u); }
__device__ __forceinline__ v4u tr_lo(const v4u* t, int i) { return v4u{pair_lo(t[0], t[1], i), pair_lo(t[2], t[3], i), pair_lo(t[4], t[5], i), pair_lo(t[6], t[7], i)}; }
__device__ __forceinline__ v4u tr_hi(const v4u* t, int i) { return v4u{pair_hi(t[0], t[1], i), pair_hi(t[2], t[3], i), pair_hi(t[4], t[5], i), pair_hi(t[6], t[7], i)}; }

template <int TB> __device__ __forceinline__ float val(u32 d) { return __uint_as_float(d & ~((1u << TB) - 1u)); }
__device__ __forceinline__ u32 tagq(float v, u32 t) { return ((__float_as_uint(v) + (1u << (QB - 1))) & ~((1u << QB) - 1u)) | t; }
__device__ __forceinline__ v4u tag12(v4f s, u32 vmask, u32 t) {
  return v4u{(__float_as_uint(s[0]) & vmask) | t, (__float_as_uint(s[1]) & vmask) | t, (__float_as_uint(s[2]) & vmask) | t, (__float_as_uint(s[3]) & vmask) | t};
}
__device__ __forceinline__ u32 differs(v4u g, u32 t) {
  return __builtin_amdgcn_bitop3_b32(g[0], g[1], t, 0x7E) | __builtin_amdgcn_bitop3_b32(g[2], g[3], t, 0x7E);
}
__device__ __forceinline__ void settle2(v4u& a, v4u& b, __amdgpu_buffer_rsrc_t r, int voff0, int voff1, int soff, u32 t12) {
  u32 n;
  asm volatile(
      "s_movk_i32 %2, 0x7fff\n"
      "1:\n\t"
      "v_bitop3_b32 v184, v176, v177, %6 bitop3:0x7e\n\t"
      "v_bitop3_b32 v185, v178, v179, %6 bitop3:0x7e\n\t"
      "v_bitop3_b32 v186, v180, v181, %6 bitop3:0x7e\n\t"
      "v_bitop3_b32 v187, v182, v183, %6 bitop3:0x7e\n\t"
      "v_or3_b32 v184, v184, v185, v186\n\t"
      "v_bitop3_b32 v184, v184, %7, v187 bitop3:0xc8\n\t"
      "v_cmp_ne_u32 vcc, 0, v184\n\t"
      "s_cbranch_vccz 2f\n\t"
      "s_and_b64 exec, exec, vcc\n\t"
      "s_sub_u32 %2, %2, 1\n\t"
      "s_cbranch_scc1 2f\n\t"
      "buffer_load_dwordx4 v[176:179], %3, %4, %5 offen sc1\n\t"
      "buffer_load_dwordx4 v[180:183], %8, %4, %5 offen sc1\n\t"
      "s_waitcnt vmcnt(0)\n\t"
      "s_branch 1b\n"
      "2:\n\t"
      "s_mov_b64 exec, -1"
      : "+{v[176:179]}"(a), "+{v[180:183]}"(b), "=&s"(n)
      : "v"(voff0), "s"(r), "s"(soff), "s"(t12), "s"(0xfffu), "v"(voff1)
      : "v184", "v185", "v186", "v187", "vcc", "scc", "memory");
}

template <int OFF> __device__ __forceinline__ void lds_arrive(u32 zero, u32 one) {
  asm volatile("s_mov_b64 exec, 1\n\tds_add_u32 %0, %1 offset:%2\n\ts_mov_b64 exec, -1" ::"v"(zero), "v"(one), "n"(OFF) : "memory");
}
template <int SLEEP> __device__ __forceinline__ void lds_wait(LAS unsigned char* p, u32 n) {
  while (__hip_atomic_load((LAS u32*)p, __ATOMIC_RELAXED, __HIP_MEMORY_SCOPE_WORKGROUP) < n) {
    if (SLEEP) __builtin_amdgcn_s_sleep(SLEEP);
  }
  asm volatile("" ::: "memory");
}
template <int OFF, int STRIDE> __device__ __forceinline__ v4f lds_wait_sum4(LAS unsigned char* lds, int o) {
  v4f a, b, c, d;
  u32 n;
  do {
    n = __hip_atomic_load((LAS u32*)(lds + OFF), __ATOMIC_RELAXED, __HIP_MEMORY_SCOPE_WORKGROUP);
    asm volatile("" ::: "memory");
    a = *(LAS v4f*)(lds + o); b = *(LAS v4f*)(lds + o + STRIDE); c = *(LAS v4f*)(lds + o + 2 * STRIDE); d = *(LAS v4f*)(lds + o + 3 * STRIDE);
    asm volatile("" : "+v"(a), "+v"(b), "+v"(c), "+v"(d)::"memory");
  } while (__builtin_amdgcn_ballot_w64(n < 4u) != 0ull);
  return (a + b) + (c + d);
}

template <int TICKS> __device__ __forceinline__ void hold(u32 t0) {
  u32 d;
  asm volatile("1:\n\ts_memrealtime vcc\n\ts_waitcnt lgkmcnt(0)\n\ts_sub_u32 %0, vcc_lo, %1\n\ts_cmpk_lt_i32 %0, %2\n\ts_cbranch_scc1 1b"
               : "=&s"(d) : "s"(t0), "n"(TICKS) : "memory", "scc", "vcc");
}
template <int STEP> __device__ __forceinline__ void pace(u32 t0) { hold<TOFF + STEP * TAU / 16>(t0); }

template <int I> struct IC { static constexpr int value = I; };
template <int B, int E, class F> __device__ __forceinline__ void static_for(F&& f) {
  if constexpr (B < E) { f(IC<B>{}); static_for<B + 1, E>(f); }
}

__global__ void __launch_bounds__(512, 2) gated_mix_decode_kernel(Args a) {
  __shared__ __attribute__((aligned(16))) unsigned char lds_raw[L_END];
  LAS unsigned char* lds = (LAS unsigned char*)lds_raw;
  const int bid = blockIdx.x, lane = threadIdx.x & 63, wave = __builtin_amdgcn_readfirstlane(threadIdx.x >> 6);
  u64 pc;
  v2p pa2;
  v4p pb4;
  asm volatile("s_getpc_b64 %2\n\ts_load_dwordx4 %0, %3, %4\n\ts_load_dwordx8 %1, %3, 0x40"
               : "=&s"(pa2), "=&s"(pb4), "=&s"(pc) : "s"(__builtin_amdgcn_kernarg_segment_ptr()), "s"((bid & 4) * 8 + (wave >> 2) * 16));
  __builtin_amdgcn_sched_barrier(0);
  const int grp = (bid & 7) * 2 + (bid >> 7), m = (bid >> 3) & 15, wg = grp * 16 + m;
  const int kvh = grp & 7, mh = m >> 2, ml = m & 3;
  const int w = wave & 3, g = lane >> 4, c = lane & 15;
  const bool vrole = wave < 4;
  const int kvbase = ((mh * 8 + kvh) * 8192 + 6144 + ml * 512) * 256, rot = (bid * 37 + w) & 127;
  const int koff = ((lane >> 2) & 3) * 256 + g * 64 + (lane & 3) * 16;
  const u32 pfc = __builtin_amdgcn_raw_buffer_load_b32(mkrsrc((const void*)(pc & ~255ull), vrole ? CODE_BYTES : 0), c * 64 + g * 65536, (ml * 4 + w) * 1024, 0);
  int vo = vrole ? g * 8 * 8192 + c * 16 : koff, n0 = vrole ? 512 * 8192 : 4 * 8 * 8192 * 256, nx = vrole ? 4096 : 0;
  const int sb = vrole ? (mh * 128 + 32 * w) * 8192 + kvh * 1024 + ml * 256 : kvbase, sr = vrole ? 0 : rot * 1024, st = vrole ? 8192 : 4096;
  int xo = ((lane >> 2) & 3) * 1024 + g * 16, xs = (mh * 128 + 32 * w) * 2, so[8];
#pragma unroll
  for (int i = 0; i < 8; ++i) so[i] = sb + ((sr + i * st) & 0x1ffff);
  u64 t64;
  asm volatile("s_waitcnt lgkmcnt(0)\n\ts_memrealtime %0"
               : "=&s"(t64), "+s"(pa2), "+s"(pb4), "+v"(vo), "+v"(xo), "+s"(n0), "+s"(nx), "+s"(xs), "+s"(so[0]), "+s"(so[1]), "+s"(so[2]),
                 "+s"(so[3]), "+s"(so[4]), "+s"(so[5]), "+s"(so[6]), "+s"(so[7]));
  const void *p0 = (const void*)pa2[0], *p1 = (const void*)pa2[1], *px = (const void*)pb4[0], *pg = (const void*)pb4[1];
  const void *po = (const void*)pb4[2], *pw = (const void*)pb4[3];
  const __amdgpu_buffer_rsrc_t rws = mkrsrc(pw, WS_END);
  const v4u xa = ld16<0>(mkrsrc(px, nx), xo, xs);
  v4u t[32];
  {
    const __amdgpu_buffer_rsrc_t r0 = mkrsrc(p0, n0);
#pragma unroll
    for (int i = 0; i < 8; ++i) t[i] = ld16<NT>(r0, vo, so[i]);
  }
  __builtin_amdgcn_sched_barrier(0);
  u32 ep;
  asm volatile("s_load_dword %0, %1, 0x0" : "=&s"(ep) : "s"(pw) : "memory");
  if (threadIdx.x < 8) *(LAS u32*)(lds + threadIdx.x * 4) = 0u;
  u32 vzero = 0u, vone = 1u, vmask = ~0xfffu;
  asm volatile("" : "+v"(vzero), "+v"(vone), "+v"(vmask));
  wg_barrier();
  asm volatile("" : "+s"(ep), "+s"(t64));
  u32 t0 = (u32)t64;
  const u32 tag = ep + 1u, t12 = tag & 0xfffu;

  if (wave < 4) {
    const __amdgpu_buffer_rsrc_t rv = mkrsrc(p1, 4 * 8 * 8192 * 256);
    const int fb = (bid & 7) >> 1, fc = (bid & 1) * 32 + (bid >> 3);
    const u32 ng = w == 2 ? 1024u : 0u;
    const v4u xv = ld16<0>(mkrsrc((const char*)px + fb * 1024, ng), lane * 16, 0), gv = ld16<0>(mkrsrc(pg, ng), lane * 16, 0);
    __builtin_amdgcn_sched_barrier(0);
    {
      float qp[8];
#pragma unroll
      for (int i = 0; i < 4; ++i) {
        qp[2 * i] = mfma(xa, tr_lo(t, i), v4f{0, 0, 0, 0})[0];
        qp[2 * i + 1] = mfma(xa, tr_hi(t, i), v4f{0, 0, 0, 0})[0];
      }
      *(LAS v4f*)(lds + L_QP + w * 2048 + lane * 32) = v4f{qp[0], qp[1], qp[2], qp[3]};
      *(LAS v4f*)(lds + L_QP + w * 2048 + lane * 32 + 16) = v4f{qp[4], qp[5], qp[6], qp[7]};
      lds_arrive<C_QP>(vzero, vone);
      if (w < 2) {
        lds_wait<0>(lds + C_QP, 4u);
        const int o = L_QP + w * 1024 + lane * 16;
        const v4f s = (*(LAS v4f*)(lds + o) + *(LAS v4f*)(lds + o + 2048)) + (*(LAS v4f*)(lds + o + 4096) + *(LAS v4f*)(lds + o + 6144));
        const u32 tq = tag & ((1u << QB) - 1u);
        __builtin_amdgcn_raw_buffer_store_b128(v4u{tagq(s[0], tq), tagq(s[1], tq), tagq(s[2], tq), tagq(s[3], tq)}, rws, lane * 16, WS_QX + wg * 2048 + w * 1024, SC1);
      }
    }
    float gate = 0.f;
    if (w == 2) {
      float z = 0.f;
#pragma unroll
      for (int i = 0; i < 4; ++i) { const u32 p = pkbf(bflo(xv[i]) * bflo(gv[i]), bfhi(xv[i]) * bfhi(gv[i])); z += bflo(p) + bfhi(p); }
      z += dpp<XOR1>(z); z += dpp<XOR2>(z); z += dpp<XOR7>(z); z += dpp<XOR8>(z);
      z = rbf(x32_sum(x16_sum(z)));
      gate = rbf(1.f / (1.f + __expf(-z)));
      asm volatile("" : "+v"(gate));
    }
    u32 pf0 = 0, pf1 = 0, pf2 = 0;
    if (w == 3) {
      hold<GWAIT>(t0);
      const v2u e = __builtin_amdgcn_raw_buffer_load_b64(rws, (lane & 7) * 8, WS_T0, SC1);
      pf0 = __builtin_amdgcn_raw_buffer_load_b32(rws, (lane & 31) * 64 + (lane >> 5) * (WS_FX - WS_OX), WS_OX + wg * 2048, 0);
      pf1 = __builtin_amdgcn_raw_buffer_load_b32(rws, 0, WS_ML + wg * 128, 0);
      pf2 = __builtin_amdgcn_raw_buffer_load_b32(mkrsrc(po, 4096), 0, (fb * 512 + fc * 8) * 2, 0);
      int dt = e[1] == tag ? (int)(e[0] - t0) : 0;
      dt = dt < -300 ? -300 : dt > 300 ? 300 : dt;
      dt += __builtin_amdgcn_mov_dpp(dt, XOR1, 0xf, 0xf, true);
      dt += __builtin_amdgcn_mov_dpp(dt, XOR2, 0xf, 0xf, true);
      dt += __builtin_amdgcn_mov_dpp(dt, XOR7, 0xf, 0xf, true);
      *(LAS u32*)(lds + L_TB) = t0 + (u32)((dt >> 3) + GOFF);
    }
    wg_barrier();
    t0 = __builtin_amdgcn_readfirstlane(*(LAS u32*)(lds + L_TB));
    v4u pa[4];
    v4f acc[8];
    float eo;
    int ow = L_OP + w * PART + g * 576 + (c & 3) * 144 + (c >> 2) * 4, orr = L_OP + (2 * w + (lane >> 5)) * 576 + ((lane >> 3) & 3) * 144 + (lane & 7) * 16;
    int src = ((lane >> 2) * 16 + (lane & 3) * 4) * 2048;
    int opub = __builtin_amdgcn_readfirstlane(WS_OX + wg * 2048 + w * 1024);
    int fsrc = __builtin_amdgcn_readfirstlane(WS_FX + (fc >> 4) * 2048 + fb * 512 + (fc & 15) * 32), odst = __builtin_amdgcn_readfirstlane((fb * 512 + fc * 8) * 2);
    float gate1 = rbf(1.f - gate);
    asm volatile("" : "+s"(opub), "+s"(fsrc), "+s"(odst), "+v"(gate1));
    const __amdgpu_buffer_rsrc_t rout = mkrsrc(po, 4096);
    static_for<0, 32>([&](auto ic) {
      constexpr int i = ic.value;
      if (i >= WIN) wait_vm<WIN - 1>();
      pace<42 + i>(t0);
      t[i] = ld16<NT>(rv, lane * 16, kvbase + ((4 * i + rot) & 127) * 1024);
      if (i == 31) __hip_atomic_store((LAS u32*)(lds + C_WO), 1u, __ATOMIC_RELAXED, __HIP_MEMORY_SCOPE_WORKGROUP);
      if (i == PV0 || i == PV1 || i == PV2) {
        constexpr int b = i == PV0 ? 0 : i == PV1 ? 1 : 2;
        __builtin_amdgcn_sched_barrier(0);
        if (b == 0) {
          lds_wait<0>(lds + C_P, 4u);
          float mw[4];
#pragma unroll
          for (int j = 0; j < 4; ++j) mw[j] = *(LAS float*)(lds + L_MLS + ((w + j) & 3) * 32 + g * 8);
#pragma unroll
          for (int j = 0; j < 4; ++j) pa[j] = *(LAS v4u*)(lds + L_P + w * 1024 + j * 256 + (lane & ~3) * 4);
          eo = __builtin_amdgcn_exp2f((mw[0] - fmaxf(fmaxf(mw[0], mw[1]), fmaxf(mw[2], mw[3]))) * LOG2E);
          asm volatile("" : "+v"(eo), "+v"(pa[1]), "+v"(pa[2]), "+v"(pa[3]), "+v"(ow), "+v"(orr), "+v"(src));
        }
#pragma unroll
        for (int j = 0; j < 4; ++j) {
          acc[2 * j] = mfma(pa[b], tr_lo(t + 8 * b, j), b ? acc[2 * j] : v4f{0, 0, 0, 0});
          acc[2 * j + 1] = mfma(pa[b], tr_hi(t + 8 * b, j), b ? acc[2 * j + 1] : v4f{0, 0, 0, 0});
        }
        __builtin_amdgcn_sched_barrier(0);
      }
    });
    asm volatile("" ::"v"(pf0), "v"(pf1), "v"(pf2), "v"(pfc));
    {
      v4u vt[8];
#pragma unroll
      for (int r = 0; r < 4; ++r) {
#pragma unroll
        for (int i = 0; i < 4; ++i) {
          vt[2 * i][r] = pair_lo(t[24 + 2 * r], t[25 + 2 * r], i);
          vt[2 * i + 1][r] = pair_hi(t[24 + 2 * r], t[25 + 2 * r], i);
        }
        if (r < 3) __builtin_amdgcn_sched_barrier(0);
      }
#pragma unroll
      for (int j = 0; j < 8; ++j) acc[j] = mfma(pa[3], vt[j], acc[j]);
    }
#pragma unroll
    for (int j = 0; j < 8; ++j) *(LAS float*)(lds + ow + j * 16) = acc[j][0] * eo;
    lds_arrive<C_O>(vzero, vone);
    if (w < 2) __builtin_amdgcn_raw_buffer_store_b128(tag12(lds_wait_sum4<C_O, PART>(lds, orr), vmask, t12), rws, lane * 16, opub, SC1);
    wg_barrier();
    if (w == 2) {
      asm volatile(".p2align 6");
      __builtin_amdgcn_s_sleep(FDELAY);
      v4u f0 = ld16<SC1>(rws, src, fsrc), f1 = ld16<SC1>(rws, src + 16, fsrc);
      settle2(f0, f1, rws, src, src + 16, fsrc, t12);
      float y[4];
#pragma unroll
      for (int i = 0; i < 4; ++i) {
        const float lo = val<12>(f0[i]), hi = val<12>(f1[i]), a0 = lo + dpp<XOR8>(lo), a1 = hi + dpp<XOR8>(hi);
        y[i] = (lane & 8) ? a1 : a0;
      }
#pragma unroll
      for (int i = 0; i < 2; ++i) {
        const float a0 = y[i] + dpp<XOR7>(y[i]), a1 = y[i + 2] + dpp<XOR7>(y[i + 2]);
        y[i] = (lane & 4) ? a1 : a0;
      }
      const float a0 = y[0] + dpp<XOR2>(y[0]), a1 = y[1] + dpp<XOR2>(y[1]);
      float s = (lane & 2) ? a1 : a0;
      s += dpp<XOR1>(s);
      s = x16_sum(s);
      const auto ab = __builtin_amdgcn_permlane32_swap(__float_as_uint(s), __float_as_uint(s), false, false);
      const u32 p = pkbf(__uint_as_float(ab[0]), __uint_as_float(ab[1]));
      const u32 r = pkbf(gate * bflo(p), gate1 * bfhi(p));
      const u32 y16 = pkbf(bflo(r) + bfhi(r), 0.f);
      if (lane < 16 && !(lane & 1)) __builtin_amdgcn_raw_buffer_store_b16((unsigned short)y16, rout, lane & 14, odst, SC1);
    }
  } else {
    const __amdgpu_buffer_rsrc_t rk = mkrsrc(p0, 4 * 8 * 8192 * 256), ro = mkrsrc(p1, 4096 * 1024);
    if (bid < 8 && w == 0 && lane == 0) __builtin_amdgcn_raw_buffer_store_b64(v2u{t0, tag}, rws, 0, WS_T0 + bid * 8, SC1);
    static_for<8, 32>([&](auto ic) {
      constexpr int i = ic.value;
      if (i >= WIN) wait_vm<WIN - 1>();
      pace<8 + i>(t0);
      t[i] = ld16<NT>(rk, koff, kvbase + ((4 * i + rot) & 127) * 1024);
    });
    const int qoff = (lane >> 5) * 8 * 2048 + (lane & 31) * 16, qsrc = WS_QX + (grp * 16 + w) * 2048 + mh * 512;
    v4u gq0 = ld16<SC1>(rws, qoff, qsrc), gq1 = ld16<SC1>(rws, qoff, qsrc + 4 * 2048);
    wg_barrier();
    t0 = __builtin_amdgcn_readfirstlane(*(LAS u32*)(lds + L_TB));
    {
      bool ok = ((differs(gq0, tag) | differs(gq1, tag)) & ((1u << QB) - 1u)) == 0u;
#pragma unroll 1
      for (int it = 0; it < SPIN_CAP && !__all(ok); ++it) {
        if (!ok) {
          gq0 = ld16<SC1>(rws, qoff, qsrc);
          gq1 = ld16<SC1>(rws, qoff, qsrc + 4 * 2048);
          ok = ((differs(gq0, tag) | differs(gq1, tag)) & ((1u << QB) - 1u)) == 0u;
        }
        asm volatile("" ::: "memory");
      }
      float qv[4];
#pragma unroll
      for (int i = 0; i < 4; ++i) qv[i] = x32_sum(val<QB>(gq0[i]) + val<QB>(gq1[i]));
      if (lane < 32) *(LAS v2u*)(lds + L_QS + w * 256 + lane * 8) = v2u{pkbf(qv[0], qv[1]), pkbf(qv[2], qv[3])};
      lds_arrive<C_QS>(vzero, vone);
      lds_wait<0>(lds + C_QS, 4u);
    }
    const v4u qb = *(LAS v4u*)(lds + L_QS + koff);
    const int q = lane & 3;
    float sc[8];
#pragma unroll
    for (int b = 0; b < 4; ++b) {
      float raw[2];
#pragma unroll
      for (int u = 0; u < 2; ++u) {
        const v4f d0 = mfma(t[8 * b + u], qb, v4f{0, 0, 0, 0}), d1 = mfma(t[8 * b + 2 + u], qb, v4f{0, 0, 0, 0});
        const v4f d2 = mfma(t[8 * b + 4 + u], qb, v4f{0, 0, 0, 0}), d3 = mfma(t[8 * b + 6 + u], qb, v4f{0, 0, 0, 0});
        const float r0 = dpp<0xFF>(d0[3]) + (dpp<0xAA>(d0[2]) + (dpp<0x55>(d0[1]) + d0[0]));
        const float r1 = dpp<0xFF>(d1[3]) + (dpp<0xAA>(d1[2]) + (dpp<0x00>(d1[0]) + d1[1]));
        const float r2 = dpp<0xFF>(d2[3]) + (dpp<0x55>(d2[1]) + (dpp<0x00>(d2[0]) + d2[2]));
        const float r3 = dpp<0xAA>(d3[2]) + (dpp<0x55>(d3[1]) + (dpp<0x00>(d3[0]) + d3[3]));
        const float s0 = (q & 1) ? r1 : r0, s1 = (q & 1) ? r3 : r2;
        raw[u] = (q & 2) ? s1 : s0;
      }
      const u32 p1 = pkbf(raw[0], raw[1]);
      const u32 p2 = pkbf(bflo(p1) * SCALE, bfhi(p1) * SCALE);
      sc[2 * b] = bflo(p2);
      sc[2 * b + 1] = bfhi(p2);
    }
    float mx = fmaxf(fmaxf(fmaxf(sc[0], sc[1]), fmaxf(sc[2], sc[3])), fmaxf(fmaxf(sc[4], sc[5]), fmaxf(sc[6], sc[7])));
    mx = fmaxf(mx, dpp<XOR1>(mx));
    mx = fmaxf(mx, dpp<XOR2>(mx));
    mx = x32_max(x16_max(mx));
    float ls = 0.f;
#pragma unroll
    for (int b = 0; b < 4; ++b) {
      const float e0 = __builtin_amdgcn_exp2f((sc[2 * b] - mx) * LOG2E), e1 = __builtin_amdgcn_exp2f((sc[2 * b + 1] - mx) * LOG2E);
      ls += e0 + e1;
      *(LAS u32*)(lds + L_P + w * 1024 + b * 256 + lane * 4) = pkbf(e0, e1);
    }
    ls += dpp<XOR1>(ls);
    ls += dpp<XOR2>(ls);
    ls = x32_sum(x16_sum(ls));
    if (lane < 16 && q == 0) *(LAS v2f*)(lds + L_MLS + w * 32 + (lane >> 2) * 8) = v2f{mx, ls};
    lds_arrive<C_P>(vzero, vone);
    if (w == 3) {
      lds_wait<1>(lds + C_P, 4u);
      float mw[4], lw[4];
#pragma unroll
      for (int i = 0; i < 4; ++i) { const v2f x = *(LAS v2f*)(lds + L_MLS + i * 32 + g * 8); mw[i] = x[0]; lw[i] = x[1]; }
      const float mg = fmaxf(fmaxf(mw[0], mw[1]), fmaxf(mw[2], mw[3]));
      float lg = 0.f;
#pragma unroll
      for (int i = 0; i < 4; ++i) lg += __builtin_amdgcn_exp2f((mw[i] - mg) * LOG2E) * lw[i];
      const u32 um = __float_as_uint(mg) | t12, ul = (__float_as_uint(lg) & vmask) | t12;
      if (c == 0) __builtin_amdgcn_raw_buffer_store_b128(v4u{um, ul, um, ul}, rws, g * 16, WS_ML + wg * 128, SC1);
    }
    if (bid == 0 && w == 0) {
      bool ok = false;
#pragma unroll 1
      for (int it = 0; it < SPIN_CAP && !__all(ok); ++it) {
        u32 d = 0;
#pragma unroll
        for (int i = 0; i < 4; ++i) d |= differs(ld16<SC1>(rws, lane * 2048, WS_QX + i * 64 * 2048), tag);
        ok = (d & ((1u << QB) - 1u)) == 0u;
        asm volatile("" ::: "memory");
      }
      if (lane == 0) __builtin_amdgcn_raw_buffer_store_b32(tag, rws, 0, WS_EPOCH, SC1);
    }
    lds_wait<1>(lds + C_WO, 1u);
    const int xa0 = 8 * w + g + 4 * (mh & 1);
    v4u gm = ld16<SC1>(rws, c * 128, WS_ML + grp * 16 * 128 + mh * 16);
    const int hb = (kvh * 4 + mh) * 128 * 1024 + ml * 256;
    int fw = L_FP + w * PART + lane * 16, fr = L_FP + (lane & 1) * PLANE + (w * 32 + (lane >> 1)) * 16, oo0 = c * 2048 + xa0 * 16, oo1 = oo0 ^ 64;
    int os = WS_OX + grp * 16 * 2048 + mh * 512, fpub = WS_FX + wg * 2048 + w * 1024;
    asm volatile("" : "+v"(fw), "+v"(fr), "+v"(oo0), "+v"(oo1), "+s"(os), "+s"(fpub));
    static_for<0, 8>([&](auto ic) {
      constexpr int i = ic.value;
      pace<74 + i>(t0);
      t[i] = ld16<NT>(ro, (i < 4 ? xa0 : xa0 ^ 4) * 1024 + c * 16, hb + 32 * (i & 3) * 1024);
    });
    lds_wait<1>(lds + C_O, 4u);
    asm volatile(".p2align 6");
    __builtin_amdgcn_s_sleep(ODELAY);
    v4u o0 = ld16<SC1>(rws, oo0, os), o1 = ld16<SC1>(rws, oo1, os);
    __builtin_amdgcn_sched_barrier(0);
    float cf;
    {
      bool ok = (differs(gm, t12) & 0xfffu) == 0u;
#pragma unroll 1
      for (int it = 0; it < SPIN_CAP && !__all(ok); ++it) {
        if (!ok) {
          gm = ld16<SC1>(rws, c * 128, WS_ML + grp * 16 * 128 + mh * 16);
          ok = (differs(gm, t12) & 0xfffu) == 0u;
        }
        asm volatile("" ::: "memory");
      }
      const float ms = val<12>(gm[0]);
      float M = fmaxf(ms, dpp<XOR1>(ms));
      M = fmaxf(M, dpp<XOR2>(M));
      const float e = __builtin_amdgcn_exp2f((ms - M) * LOG2E);
      float L = e * val<12>(gm[1]);
      L += dpp<XOR1>(L);
      L += dpp<XOR2>(L);
      cf = e * __builtin_amdgcn_rcpf(L);
    }
    asm volatile("" : "+v"(cf));
    __builtin_amdgcn_sched_barrier(0);
    v4u wt[8];
#pragma unroll
    for (int r = 0; r < 4; ++r)
#pragma unroll
      for (int i = 0; i < 4; ++i) {
        u32 lo = pair_lo(t[2 * r], t[2 * r + 1], i), hi = pair_hi(t[2 * r], t[2 * r + 1], i);
        asm volatile("" : "+v"(lo), "+v"(hi));
        wt[2 * i][r] = lo;
        wt[2 * i + 1][r] = hi;
      }
    __builtin_amdgcn_sched_barrier(0);
    settle2(o0, o1, rws, oo0, oo1, os, t12);
    float ov[8];
#pragma unroll
    for (int i = 0; i < 4; ++i) { ov[i] = cf * val<12>(o0[i]); ov[4 + i] = cf * val<12>(o1[i]); }
#define QADD(i, p) "v_add_f32_dpp %" #i ", %" #i ", %" #i " quad_perm:" p " row_mask:0xf bank_mask:0xf\n\t"
#define QADD8(p) QADD(0, p) QADD(1, p) QADD(2, p) QADD(3, p) QADD(4, p) QADD(5, p) QADD(6, p) QADD(7, p)
    asm volatile("s_nop 1\n\t" QADD8("[1,0,3,2]") QADD8("[2,3,0,1]")
                 : "+v"(ov[0]), "+v"(ov[1]), "+v"(ov[2]), "+v"(ov[3]), "+v"(ov[4]), "+v"(ov[5]), "+v"(ov[6]), "+v"(ov[7]));
    const v4u oa = {pkbf(ov[0], ov[1]), pkbf(ov[2], ov[3]), pkbf(ov[4], ov[5]), pkbf(ov[6], ov[7])};
    float f[8];
#pragma unroll
    for (int j = 0; j < 8; ++j) f[j] = mfma(oa, wt[j], v4f{0, 0, 0, 0})[0];
    *(LAS v4f*)(lds + fw) = v4f{f[0], f[1], f[2], f[3]};
    *(LAS v4f*)(lds + fw + PLANE) = v4f{f[4], f[5], f[6], f[7]};
    wg_barrier();
    if (w < 2) {
      const v4f s = (*(LAS v4f*)(lds + fr) + *(LAS v4f*)(lds + fr + PART)) + (*(LAS v4f*)(lds + fr + 2 * PART) + *(LAS v4f*)(lds + fr + 3 * PART));
      __builtin_amdgcn_raw_buffer_store_b128(tag12(s, vmask, t12), rws, lane * 16, fpub, SC1);
    }
  }
}

extern "C" void kernel_launch(void* const* d_in, const int* in_sizes, int n_in, void* d_out, int out_size, void* d_ws, size_t ws_size,
                              hipStream_t stream) {
  Args a;
  a.p[0] = d_in[3]; a.p[1] = d_in[2]; a.p[2] = d_in[1]; a.p[3] = d_in[4];
  a.p[4] = d_in[8]; a.p[5] = d_in[7]; a.p[6] = d_in[6]; a.p[7] = d_in[9];
  a.x = d_in[0];
  a.gw = d_in[10];
  a.out = d_out;
  a.ws = d_ws;
  gated_mix_decode_kernel<<<256, 512, 0, stream>>>(a);
}
```

```cpp
#include <hip/hip_runtime.h>
#include <hip/hip_bf16.h>

typedef unsigned u32;
typedef unsigned long long u64;
typedef u32 v4u __attribute__((ext_vector_type(4)));
typedef u32 v2u __attribute__((ext_vector_type(2)));
typedef float v4f __attribute__((ext_vector_type(4)));
typedef float v2f __attribute__((ext_vector_type(2)));
typedef u64 v4p __attribute__((ext_vector_type(4)));
typedef __bf16 v8b __attribute__((ext_vector_type(8)));
typedef __bf16 v2b __attribute__((ext_vector_type(2)));
#define LAS __attribute__((address_space(3)))

constexpr int TAU = 246;
constexpr int TOFF = -60;
constexpr int GOFF = 10;
constexpr int GWAIT = 300;
constexpr int WIN = 16;
constexpr int PV0 = 21, PV1 = 25, PV2 = 31;
constexpr int ODELAY = 11, FDELAY = 24;
constexpr int CODE_BYTES = 13532;

constexpr int NT = 2, SC1 = 16;
constexpr u32 WS_EPOCH = 0;
constexpr u32 WS_T0 = 256;
constexpr u32 WS_QX = 4096;
constexpr u32 WS_OX = WS_QX + 256 * 2048;
constexpr u32 WS_ML = WS_OX + 256 * 2048;
constexpr u32 WS_FX = WS_ML + 256 * 128;
constexpr u32 WS_END = WS_FX + 256 * 2048;
constexpr int QB = 9;
constexpr int SPIN_CAP = 1 << 15;
constexpr float LOG2E = 1.4426950408889634f, SCALE = 0.08838834764831845f;

constexpr int C_QP = 0, C_QS = 4, C_WO = 8, C_P = 12, C_O = 16, L_TB = 32;
constexpr int L_MLS = 64, L_QS = 256, L_P = L_QS + 1024, L_QP = L_P + 4096;
constexpr int PLANE = 1024 + 128, PART = 2 * PLANE;
constexpr int L_OP = L_QP + 4 * 2048, L_FP = L_OP + 4 * PART, L_END = L_FP + 4 * PART;

struct Args {
  const void* first[4];
  const void* x;
  void* ws;
  const void* gw;
  void* out;
  const void* second[4];
};

__device__ __forceinline__ __amdgpu_buffer_rsrc_t mkrsrc(const void* p, u32 bytes) {
  return __builtin_amdgcn_make_buffer_rsrc((void*)p, 0, bytes, 0x00020000);
}
template <int AUX> __device__ __forceinline__ v4u ld16(__amdgpu_buffer_rsrc_t r, int voff, int soff) {
  return __builtin_amdgcn_raw_buffer_load_b128(r, voff, soff, AUX);
}
template <int N> __device__ __forceinline__ void wait_vm() { asm volatile("s_waitcnt vmcnt(%0)" ::"n"(N) : "memory"); }
__device__ __forceinline__ void wg_barrier() {
  __builtin_amdgcn_sched_barrier(0);
  asm volatile("s_waitcnt lgkmcnt(0)\n\ts_barrier" ::: "memory");
  __builtin_amdgcn_sched_barrier(0);
}
template <int CTRL> __device__ __forceinline__ float dpp(float x) {
  return __builtin_bit_cast(float, __builtin_amdgcn_mov_dpp(__builtin_bit_cast(int, x), CTRL, 0xf, 0xf, true));
}
constexpr int XOR1 = 0xB1, XOR2 = 0x4E, XOR7 = 0x141, XOR8 = 0x128;
__device__ __forceinline__ float x16_sum(float x) {
  auto s = __builtin_amdgcn_permlane16_swap(__float_as_uint(x), __float_as_uint(x), false, false);
  return __uint_as_float(s[0]) + __uint_as_float(s[1]);
}
__device__ __forceinline__ float x32_sum(float x) {
  auto s = __builtin_amdgcn_permlane32_swap(__float_as_uint(x), __float_as_uint(x), false, false);
  return __uint_as_float(s[0]) + __uint_as_float(s[1]);
}
__device__ __forceinline__ float x16_max(float x) {
  auto s = __builtin_amdgcn_permlane16_swap(__float_as_uint(x), __float_as_uint(x), false, false);
  return fmaxf(__uint_as_float(s[0]), __uint_as_float(s[1]));
}
__device__ __forceinline__ float x32_max(float x) {
  auto s = __builtin_amdgcn_permlane32_swap(__float_as_uint(x), __float_as_uint(x), false, false);
  return fmaxf(__uint_as_float(s[0]), __uint_as_float(s[1]));
}
__device__ __forceinline__ u32 pkbf(float lo, float hi) {
  v2b r = {(__bf16)lo, (__bf16)hi};
  return __builtin_bit_cast(u32, r);
}
__device__ __forceinline__ float bflo(u32 p) { return __uint_as_float(p << 16); }
__device__ __forceinline__ float bfhi(u32 p) { return __uint_as_float(p & 0xffff0000u); }
__device__ __forceinline__ float rbf(float x) { return bflo(pkbf(x, x)); }
__device__ __forceinline__ v4f mfma(v4u a, v4u b, v4f c) {
  return __builtin_amdgcn_mfma_f32_16x16x32_bf16(__builtin_bit_cast(v8b, a), __builtin_bit_cast(v8b, b), c, 0, 0, 0);
}
__device__ __forceinline__ u32 pair_lo(v4u a, v4u b, int i) { return __builtin_amdgcn_perm(b[i], a[i], 0x05040100u); }
__device__ __forceinline__ u32 pair_hi(v4u a, v4u b, int i) { return __builtin_amdgcn_perm(b[i], a[i], 0x07060302u); }
__device__ __forceinline__ v4u tr_lo(const v4u* t, int i) { return v4u{pair_lo(t[0], t[1], i), pair_lo(t[2], t[3], i), pair_lo(t[4], t[5], i), pair_lo(t[6], t[7], i)}; }
__device__ __forceinline__ v4u tr_hi(const v4u* t, int i) { return v4u{pair_hi(t[0], t[1], i), pair_hi(t[2], t[3], i), pair_hi(t[4], t[5], i), pair_hi(t[6], t[7], i)}; }

template <int TB> __device__ __forceinline__ float val(u32 d) { return __uint_as_float(d & ~((1u << TB) - 1u)); }
__device__ __forceinline__ u32 tagq(float v, u32 t) { return ((__float_as_uint(v) + (1u << (QB - 1))) & ~((1u << QB) - 1u)) | t; }
__device__ __forceinline__ v4u tag12(v4f s, u32 vmask, u32 t) {
  return v4u{(__float_as_uint(s[0]) & vmask) | t, (__float_as_uint(s[1]) & vmask) | t, (__float_as_uint(s[2]) & vmask) | t, (__float_as_uint(s[3]) & vmask) | t};
}
__device__ __forceinline__ u32 differs(v4u g, u32 t) {
  return __builtin_amdgcn_bitop3_b32(g[0], g[1], t, 0x7E) | __builtin_amdgcn_bitop3_b32(g[2], g[3], t, 0x7E);
}
__device__ __forceinline__ void settle2(v4u& a, v4u& b, __amdgpu_buffer_rsrc_t r, int voff0, int voff1, int soff, u32 t12) {
  u32 n;
  asm volatile(
      "s_movk_i32 %2, 0x7fff\n"
      "1:\n\t"
      "v_bitop3_b32 v184, v176, v177, %6 bitop3:0x7e\n\t"
      "v_bitop3_b32 v185, v178, v179, %6 bitop3:0x7e\n\t"
      "v_bitop3_b32 v186, v180, v181, %6 bitop3:0x7e\n\t"
      "v_bitop3_b32 v187, v182, v183, %6 bitop3:0x7e\n\t"
      "v_or3_b32 v184, v184, v185, v186\n\t"
      "v_bitop3_b32 v184, v184, %7, v187 bitop3:0xc8\n\t"
      "v_cmp_ne_u32 vcc, 0, v184\n\t"
      "s_cbranch_vccz 2f\n\t"
      "s_and_b64 exec, exec, vcc\n\t"
      "s_sub_u32 %2, %2, 1\n\t"
      "s_cbranch_scc1 2f\n\t"
      "buffer_load_dwordx4 v[176:179], %3, %4, %5 offen sc1\n\t"
      "buffer_load_dwordx4 v[180:183], %8, %4, %5 offen sc1\n\t"
      "s_waitcnt vmcnt(0)\n\t"
      "s_branch 1b\n"
      "2:\n\t"
      "s_mov_b64 exec, -1"
      : "+{v[176:179]}"(a), "+{v[180:183]}"(b), "=&s"(n)
      : "v"(voff0), "s"(r), "s"(soff), "s"(t12), "s"(0xfffu), "v"(voff1)
      : "v184", "v185", "v186", "v187", "vcc", "scc", "memory");
}

template <int OFF> __device__ __forceinline__ void lds_arrive(u32 zero, u32 one) {
  asm volatile("s_mov_b64 exec, 1\n\tds_add_u32 %0, %1 offset:%2\n\ts_mov_b64 exec, -1" ::"v"(zero), "v"(one), "n"(OFF) : "memory");
}
template <int SLEEP> __device__ __forceinline__ void lds_wait(LAS unsigned char* p, u32 n) {
  while (__hip_atomic_load((LAS u32*)p, __ATOMIC_RELAXED, __HIP_MEMORY_SCOPE_WORKGROUP) < n) {
    if (SLEEP) __builtin_amdgcn_s_sleep(SLEEP);
  }
  asm volatile("" ::: "memory");
}
template <int OFF, int STRIDE> __device__ __forceinline__ v4f lds_wait_sum4(LAS unsigned char* lds, int o) {
  v4f a, b, c, d;
  u32 n;
  do {
    n = __hip_atomic_load((LAS u32*)(lds + OFF), __ATOMIC_RELAXED, __HIP_MEMORY_SCOPE_WORKGROUP);
    asm volatile("" ::: "memory");
    a = *(LAS v4f*)(lds + o); b = *(LAS v4f*)(lds + o + STRIDE); c = *(LAS v4f*)(lds + o + 2 * STRIDE); d = *(LAS v4f*)(lds + o + 3 * STRIDE);
    asm volatile("" : "+v"(a), "+v"(b), "+v"(c), "+v"(d)::"memory");
  } while (__builtin_amdgcn_ballot_w64(n < 4u) != 0ull);
  return (a + b) + (c + d);
}

template <int TICKS> __device__ __forceinline__ void hold(u32 t0) {
  u32 d;
  asm volatile("1:\n\ts_memrealtime vcc\n\ts_waitcnt lgkmcnt(0)\n\ts_sub_u32 %0, vcc_lo, %1\n\ts_cmpk_lt_i32 %0, %2\n\ts_cbranch_scc1 1b"
               : "=&s"(d) : "s"(t0), "n"(TICKS) : "memory", "scc", "vcc");
}
template <int STEP> __device__ __forceinline__ void pace(u32 t0) { hold<TOFF + STEP * TAU / 16>(t0); }

template <int I> struct IC { static constexpr int value = I; };
template <int B, int E, class F> __device__ __forceinline__ void static_for(F&& f) {
  if constexpr (B < E) { f(IC<B>{}); static_for<B + 1, E>(f); }
}

__global__ void __launch_bounds__(512, 2) gated_mix_decode_kernel(Args a) {
  __shared__ __attribute__((aligned(16))) unsigned char lds_raw[L_END];
  LAS unsigned char* lds = (LAS unsigned char*)lds_raw;
  const int bid = blockIdx.x, lane = threadIdx.x & 63, wave = __builtin_amdgcn_readfirstlane(threadIdx.x >> 6);
  u64 pc;
  u64 pa0, pa1;
  v4p pb4;
  const int aoff = (bid & 4) * 4 + (wave >> 2) * 8;
  asm volatile("s_getpc_b64 %2\n\ts_load_dwordx2 %0, %3, %4\n\ts_load_dwordx8 %1, %3, 0x20"
               : "=&s"(pa0), "=&s"(pb4), "=&s"(pc) : "s"(__builtin_amdgcn_kernarg_segment_ptr()), "s"(aoff));
  __builtin_amdgcn_sched_barrier(0);
  const int grp = (bid & 7) * 2 + (bid >> 7), m = (bid >> 3) & 15, wg = grp * 16 + m;
  const int kvh = grp & 7, mh = m >> 2, ml = m & 3;
  const int w = wave & 3, g = lane >> 4, c = lane & 15;
  const bool vrole = wave < 4;
  const int kvbase = ((mh * 8 + kvh) * 8192 + 6144 + ml * 512) * 256, rot = (bid * 37 + w) & 127;
  const int koff = ((lane >> 2) & 3) * 256 + g * 64 + (lane & 3) * 16;
  const u32 pfc = __builtin_amdgcn_raw_buffer_load_b32(mkrsrc((const void*)(pc & ~255ull), vrole ? CODE_BYTES : 0), c * 64 + g * 65536, (ml * 4 + w) * 1024, 0);
  int vo = vrole ? g * 8 * 8192 + c * 16 : koff, n0 = vrole ? 512 * 8192 : 4 * 8 * 8192 * 256, nx = vrole ? 4096 : 0;
  const int sb = vrole ? (mh * 128 + 32 * w) * 8192 + kvh * 1024 + ml * 256 : kvbase, sr = vrole ? 0 : rot * 1024, st = vrole ? 8192 : 4096;
  int xo = ((lane >> 2) & 3) * 1024 + g * 16, xs = (mh * 128 + 32 * w) * 2, so[8];
#pragma unroll
  for (int i = 0; i < 8; ++i) so[i] = sb + ((sr + i * st) & 0x1ffff);
  u64 t64;
  asm volatile("s_waitcnt lgkmcnt(0)\n\ts_memrealtime %0"
               : "=&s"(t64), "+s"(pa0), "+s"(pb4), "+v"(vo), "+v"(xo), "+s"(n0), "+s"(nx), "+s"(xs), "+s"(so[0]), "+s"(so[1]), "+s"(so[2]),
                 "+s"(so[3]), "+s"(so[4]), "+s"(so[5]), "+s"(so[6]), "+s"(so[7]));
  const void *p0 = (const void*)pa0, *px = (const void*)pb4[0], *pw = (const void*)pb4[1], *pg = (const void*)pb4[2], *po = (const void*)pb4[3];
  const __amdgpu_buffer_rsrc_t rws = mkrsrc(pw, WS_END);
  const v4u xa = ld16<0>(mkrsrc(px, nx), xo, xs);
  v4u t[32];
  {
    const __amdgpu_buffer_rsrc_t r0 = mkrsrc(p0, n0);
#pragma unroll
    for (int i = 0; i < 8; ++i) t[i] = ld16<NT>(r0, vo, so[i]);
  }
  __builtin_amdgcn_sched_barrier(0);
  u32 ep;
  asm volatile("s_load_dword %0, %2, 0x0\n\ts_load_dwordx2 %1, %3, %4"
               : "=&s"(ep), "=&s"(pa1) : "s"(pw), "s"(__builtin_amdgcn_kernarg_segment_ptr()), "s"(aoff + 64) : "memory");
  if (threadIdx.x < 8) *(LAS u32*)(lds + threadIdx.x * 4) = 0u;
  u32 vzero = 0u, vone = 1u, vmask = ~0xfffu;
  asm volatile("" : "+v"(vzero), "+v"(vone), "+v"(vmask));
  wg_barrier();
  asm volatile("" : "+s"(ep), "+s"(t64), "+s"(pa1));
  const void* p1 = (const void*)pa1;
  u32 t0 = (u32)t64;
  const u32 tag = ep + 1u, t12 = tag & 0xfffu;

  if (wave < 4) {
    const __amdgpu_buffer_rsrc_t rv = mkrsrc(p1, 4 * 8 * 8192 * 256);
    const int fb = (bid & 7) >> 1, fc = (bid & 1) * 32 + (bid >> 3);
    const u32 ng = w == 2 ? 1024u : 0u;
    const v4u xv = ld16<0>(mkrsrc((const char*)px + fb * 1024, ng), lane * 16, 0), gv = ld16<0>(mkrsrc(pg, ng), lane * 16, 0);
    __builtin_amdgcn_sched_barrier(0);
    {
      float qp[8];
#pragma unroll
      for (int i = 0; i < 4; ++i) {
        qp[2 * i] = mfma(xa, tr_lo(t, i), v4f{0, 0, 0, 0})[0];
        qp[2 * i + 1] = mfma(xa, tr_hi(t, i), v4f{0, 0, 0, 0})[0];
      }
      *(LAS v4f*)(lds + L_QP + w * 2048 + lane * 32) = v4f{qp[0], qp[1], qp[2], qp[3]};
      *(LAS v4f*)(lds + L_QP + w * 2048 + lane * 32 + 16) = v4f{qp[4], qp[5], qp[6], qp[7]};
      lds_arrive<C_QP>(vzero, vone);
      if (w < 2) {
        lds_wait<0>(lds + C_QP, 4u);
        const int o = L_QP + w * 1024 + lane * 16;
        const v4f s = (*(LAS v4f*)(lds + o) + *(LAS v4f*)(lds + o + 2048)) + (*(LAS v4f*)(lds + o + 4096) + *(LAS v4f*)(lds + o + 6144));
        const u32 tq = tag & ((1u << QB) - 1u);
        __builtin_amdgcn_raw_buffer_store_b128(v4u{tagq(s[0], tq), tagq(s[1], tq), tagq(s[2], tq), tagq(s[3], tq)}, rws, lane * 16, WS_QX + wg * 2048 + w * 1024, SC1);
      }
    }
    float gate = 0.f;
    if (w == 2) {
      float z = 0.f;
#pragma unroll
      for (int i = 0; i < 4; ++i) { const u32 p = pkbf(bflo(xv[i]) * bflo(gv[i]), bfhi(xv[i]) * bfhi(gv[i])); z += bflo(p) + bfhi(p); }
      z += dpp<XOR1>(z); z += dpp<XOR2>(z); z += dpp<XOR7>(z); z += dpp<XOR8>(z);
      z = rbf(x32_sum(x16_sum(z)));
      gate = rbf(1.f / (1.f + __expf(-z)));
      asm volatile("" : "+v"(gate));
    }
    u32 pf0 = 0, pf1 = 0, pf2 = 0;
    if (w == 3) {
      hold<GWAIT>(t0);
      const v2u e = __builtin_amdgcn_raw_buffer_load_b64(rws, (lane & 7) * 8, WS_T0, SC1);
      pf0 = __builtin_amdgcn_raw_buffer_load_b32(rws, (lane & 31) * 64 + (lane >> 5) * (WS_FX - WS_OX), WS_OX + wg * 2048, 0);
      pf1 = __builtin_amdgcn_raw_buffer_load_b32(rws, 0, WS_ML + wg * 128, 0);
      pf2 = __builtin_amdgcn_raw_buffer_load_b32(mkrsrc(po, 4096), 0, (fb * 512 + fc * 8) * 2, 0);
      int dt = e[1] == tag ? (int)(e[0] - t0) : 0;
      dt = dt < -300 ? -300 : dt > 300 ? 300 : dt;
      dt += __builtin_amdgcn_mov_dpp(dt, XOR1, 0xf, 0xf, true);
      dt += __builtin_amdgcn_mov_dpp(dt, XOR2, 0xf, 0xf, true);
      dt += __builtin_amdgcn_mov_dpp(dt, XOR7, 0xf, 0xf, true);
      *(LAS u32*)(lds + L_TB) = t0 + (u32)((dt >> 3) + GOFF);
    }
    wg_barrier();
    t0 = __builtin_amdgcn_readfirstlane(*(LAS u32*)(lds + L_TB));
    v4u pa[4];
    v4f acc[8];
    float eo;
    int ow = L_OP + w * PART + g * 576 + (c & 3) * 144 + (c >> 2) * 4, orr = L_OP + (2 * w + (lane >> 5)) * 576 + ((lane >> 3) & 3) * 144 + (lane & 7) * 16;
    int src = ((lane >> 2) * 16 + (lane & 3) * 4) * 2048;
    int opub = __builtin_amdgcn_readfirstlane(WS_OX + wg * 2048 + w * 1024);
    int fsrc = __builtin_amdgcn_readfirstlane(WS_FX + (fc >> 4) * 2048 + fb * 512 + (fc & 15) * 32), odst = __builtin_amdgcn_readfirstlane((fb * 512 + fc * 8) * 2);
    float gate1 = rbf(1.f - gate);
    asm volatile("" : "+s"(opub), "+s"(fsrc), "+s"(odst), "+v"(gate1));
    const __amdgpu_buffer_rsrc_t rout = mkrsrc(po, 4096);
    static_for<0, 32>([&](auto ic) {
      constexpr int i = ic.value;
      if (i >= WIN) wait_vm<WIN - 1>();
      pace<42 + i>(t0);
      t[i] = ld16<NT>(rv, lane * 16, kvbase + ((4 * i + rot) & 127) * 1024);
      if (i == 31) __hip_atomic_store((LAS u32*)(lds + C_WO), 1u, __ATOMIC_RELAXED, __HIP_MEMORY_SCOPE_WORKGROUP);
      if (i == PV0 || i == PV1 || i == PV2) {
        constexpr int b = i == PV0 ? 0 : i == PV1 ? 1 : 2;
        __builtin_amdgcn_sched_barrier(0);
        if (b == 0) {
          lds_wait<0>(lds + C_P, 4u);
          float mw[4];
#pragma unroll
          for (int j = 0; j < 4; ++j) mw[j] = *(LAS float*)(lds + L_MLS + ((w + j) & 3) * 32 + g * 8);
#pragma unroll
          for (int j = 0; j < 4; ++j) pa[j] = *(LAS v4u*)(lds + L_P + w * 1024 + j * 256 + (lane & ~3) * 4);
          eo = __builtin_amdgcn_exp2f((mw[0] - fmaxf(fmaxf(mw[0], mw[1]), fmaxf(mw[2], mw[3]))) * LOG2E);
          asm volatile("" : "+v"(eo), "+v"(pa[1]), "+v"(pa[2]), "+v"(pa[3]), "+v"(ow), "+v"(orr), "+v"(src));
        }
#pragma unroll
        for (int j = 0; j < 4; ++j) {
          acc[2 * j] = mfma(pa[b], tr_lo(t + 8 * b, j), b ? acc[2 * j] : v4f{0, 0, 0, 0});
          acc[2 * j + 1] = mfma(pa[b], tr_hi(t + 8 * b, j), b ? acc[2 * j + 1] : v4f{0, 0, 0, 0});
        }
        __builtin_amdgcn_sched_barrier(0);
      }
    });
    asm volatile("" ::"v"(pf0), "v"(pf1), "v"(pf2), "v"(pfc));
    {
      v4u vt[8];
#pragma unroll
      for (int r = 0; r < 4; ++r) {
#pragma unroll
        for (int i = 0; i < 4; ++i) {
          vt[2 * i][r] = pair_lo(t[24 + 2 * r], t[25 + 2 * r], i);
          vt[2 * i + 1][r] = pair_hi(t[24 + 2 * r], t[25 + 2 * r], i);
        }
        if (r < 3) __builtin_amdgcn_sched_barrier(0);
      }
#pragma unroll
      for (int j = 0; j < 8; ++j) acc[j] = mfma(pa[3], vt[j], acc[j]);
    }
#pragma unroll
    for (int j = 0; j < 8; ++j) *(LAS float*)(lds + ow + j * 16) = acc[j][0] * eo;
    lds_arrive<C_O>(vzero, vone);
    if (w < 2) __builtin_amdgcn_raw_buffer_store_b128(tag12(lds_wait_sum4<C_O, PART>(lds, orr), vmask, t12), rws, lane * 16, opub, SC1);
    wg_barrier();
    if (w == 2) {
      asm volatile(".p2align 6");
      __builtin_amdgcn_s_sleep(FDELAY);
      v4u f0 = ld16<SC1>(rws, src, fsrc), f1 = ld16<SC1>(rws, src + 16, fsrc);
      settle2(f0, f1, rws, src, src + 16, fsrc, t12);
      float y[4];
#pragma unroll
      for (int i = 0; i < 4; ++i) {
        const float lo = val<12>(f0[i]), hi = val<12>(f1[i]), a0 = lo + dpp<XOR8>(lo), a1 = hi + dpp<XOR8>(hi);
        y[i] = (lane & 8) ? a1 : a0;
      }
#pragma unroll
      for (int i = 0; i < 2; ++i) {
        const float a0 = y[i] + dpp<XOR7>(y[i]), a1 = y[i + 2] + dpp<XOR7>(y[i + 2]);
        y[i] = (lane & 4) ? a1 : a0;
      }
      const float a0 = y[0] + dpp<XOR2>(y[0]), a1 = y[1] + dpp<XOR2>(y[1]);
      float s = (lane & 2) ? a1 : a0;
      s += dpp<XOR1>(s);
      s = x16_sum(s);
      const auto ab = __builtin_amdgcn_permlane32_swap(__float_as_uint(s), __float_as_uint(s), false, false);
      const u32 p = pkbf(__uint_as_float(ab[0]), __uint_as_float(ab[1]));
      const u32 r = pkbf(gate * bflo(p), gate1 * bfhi(p));
      const u32 y16 = pkbf(bflo(r) + bfhi(r), 0.f);
      if (lane < 16 && !(lane & 1)) __builtin_amdgcn_raw_buffer_store_b16((unsigned short)y16, rout, lane & 14, odst, SC1);
    }
  } else {
    const __amdgpu_buffer_rsrc_t rk = mkrsrc(p0, 4 * 8 * 8192 * 256), ro = mkrsrc(p1, 4096 * 1024);
    if (bid < 8 && w == 0 && lane == 0) __builtin_amdgcn_raw_buffer_store_b64(v2u{t0, tag}, rws, 0, WS_T0 + bid * 8, SC1);
    static_for<8, 32>([&](auto ic) {
      constexpr int i = ic.value;
      if (i >= WIN) wait_vm<WIN - 1>();
      pace<8 + i>(t0);
      t[i] = ld16<NT>(rk, koff, kvbase + ((4 * i + rot) & 127) * 1024);
    });
    const int qoff = (lane >> 5) * 8 * 2048 + (lane & 31) * 16, qsrc = WS_QX + (grp * 16 + w) * 2048 + mh * 512;
    v4u gq0 = ld16<SC1>(rws, qoff, qsrc), gq1 = ld16<SC1>(rws, qoff, qsrc + 4 * 2048);
    wg_barrier();
    t0 = __builtin_amdgcn_readfirstlane(*(LAS u32*)(lds + L_TB));
    {
      bool ok = ((differs(gq0, tag) | differs(gq1, tag)) & ((1u << QB) - 1u)) == 0u;
#pragma unroll 1
      for (int it = 0; it < SPIN_CAP && !__all(ok); ++it) {
        if (!ok) {
          gq0 = ld16<SC1>(rws, qoff, qsrc);
          gq1 = ld16<SC1>(rws, qoff, qsrc + 4 * 2048);
          ok = ((differs(gq0, tag) | differs(gq1, tag)) & ((1u << QB) - 1u)) == 0u;
        }
        asm volatile("" ::: "memory");
      }
      float qv[4];
#pragma unroll
      for (int i = 0; i < 4; ++i) qv[i] = x32_sum(val<QB>(gq0[i]) + val<QB>(gq1[i]));
      if (lane < 32) *(LAS v2u*)(lds + L_QS + w * 256 + lane * 8) = v2u{pkbf(qv[0], qv[1]), pkbf(qv[2], qv[3])};
      lds_arrive<C_QS>(vzero, vone);
      lds_wait<0>(lds + C_QS, 4u);
    }
    const v4u qb = *(LAS v4u*)(lds + L_QS + koff);
    const int q = lane & 3;
    float sc[8];
#pragma unroll
    for (int b = 0; b < 4; ++b) {
      float raw[2];
#pragma unroll
      for (int u = 0; u < 2; ++u) {
        const v4f d0 = mfma(t[8 * b + u], qb, v4f{0, 0, 0, 0}), d1 = mfma(t[8 * b + 2 + u], qb, v4f{0, 0, 0, 0});
        const v4f d2 = mfma(t[8 * b + 4 + u], qb, v4f{0, 0, 0, 0}), d3 = mfma(t[8 * b + 6 + u], qb, v4f{0, 0, 0, 0});
        const float r0 = dpp<0xFF>(d0[3]) + (dpp<0xAA>(d0[2]) + (dpp<0x55>(d0[1]) + d0[0]));
        const float r1 = dpp<0xFF>(d1[3]) + (dpp<0xAA>(d1[2]) + (dpp<0x00>(d1[0]) + d1[1]));
        const float r2 = dpp<0xFF>(d2[3]) + (dpp<0x55>(d2[1]) + (dpp<0x00>(d2[0]) + d2[2]));
        const float r3 = dpp<0xAA>(d3[2]) + (dpp<0x55>(d3[1]) + (dpp<0x00>(d3[0]) + d3[3]));
        const float s0 = (q & 1) ? r1 : r0, s1 = (q & 1) ? r3 : r2;
        raw[u] = (q & 2) ? s1 : s0;
      }
      const u32 p1 = pkbf(raw[0], raw[1]);
      const u32 p2 = pkbf(bflo(p1) * SCALE, bfhi(p1) * SCALE);
      sc[2 * b] = bflo(p2);
      sc[2 * b + 1] = bfhi(p2);
    }
    float mx = fmaxf(fmaxf(fmaxf(sc[0], sc[1]), fmaxf(sc[2], sc[3])), fmaxf(fmaxf(sc[4], sc[5]), fmaxf(sc[6], sc[7])));
    mx = fmaxf(mx, dpp<XOR1>(mx));
    mx = fmaxf(mx, dpp<XOR2>(mx));
    mx = x32_max(x16_max(mx));
    float ls = 0.f;
#pragma unroll
    for (int b = 0; b < 4; ++b) {
      const float e0 = __builtin_amdgcn_exp2f((sc[2 * b] - mx) * LOG2E), e1 = __builtin_amdgcn_exp2f((sc[2 * b + 1] - mx) * LOG2E);
      ls += e0 + e1;
      *(LAS u32*)(lds + L_P + w * 1024 + b * 256 + lane * 4) = pkbf(e0, e1);
    }
    ls += dpp<XOR1>(ls);
    ls += dpp<XOR2>(ls);
    ls = x32_sum(x16_sum(ls));
    if (lane < 16 && q == 0) *(LAS v2f*)(lds + L_MLS + w * 32 + (lane >> 2) * 8) = v2f{mx, ls};
    lds_arrive<C_P>(vzero, vone);
    if (w == 3) {
      lds_wait<1>(lds + C_P, 4u);
      float mw[4], lw[4];
#pragma unroll
      for (int i = 0; i < 4; ++i) { const v2f x = *(LAS v2f*)(lds + L_MLS + i * 32 + g * 8); mw[i] = x[0]; lw[i] = x[1]; }
      const float mg = fmaxf(fmaxf(mw[0], mw[1]), fmaxf(mw[2], mw[3]));
      float lg = 0.f;
#pragma unroll
      for (int i = 0; i < 4; ++i) lg += __builtin_amdgcn_exp2f((mw[i] - mg) * LOG2E) * lw[i];
      const u32 um = __float_as_uint(mg) | t12, ul = (__float_as_uint(lg) & vmask) | t12;
      if (c == 0) __builtin_amdgcn_raw_buffer_store_b128(v4u{um, ul, um, ul}, rws, g * 16, WS_ML + wg * 128, SC1);
    }
    if (bid == 0 && w == 0) {
      bool ok = false;
#pragma unroll 1
      for (int it = 0; it < SPIN_CAP && !__all(ok); ++it) {
        u32 d = 0;
#pragma unroll
        for (int i = 0; i < 4; ++i) d |= differs(ld16<SC1>(rws, lane * 2048, WS_QX + i * 64 * 2048), tag);
        ok = (d & ((1u << QB) - 1u)) == 0u;
        asm volatile("" ::: "memory");
      }
      if (lane == 0) __builtin_amdgcn_raw_buffer_store_b32(tag, rws, 0, WS_EPOCH, SC1);
    }
    lds_wait<1>(lds + C_WO, 1u);
    const int xa0 = 8 * w + g + 4 * (mh & 1);
    v4u gm = ld16<SC1>(rws, c * 128, WS_ML + grp * 16 * 128 + mh * 16);
    const int hb = (kvh * 4 + mh) * 128 * 1024 + ml * 256;
    int fw = L_FP + w * PART + lane * 16, fr = L_FP + (lane & 1) * PLANE + (w * 32 + (lane >> 1)) * 16, oo0 = c * 2048 + xa0 * 16, oo1 = oo0 ^ 64;
    int os = WS_OX + grp * 16 * 2048 + mh * 512, fpub = WS_FX + wg * 2048 + w * 1024;
    asm volatile("" : "+v"(fw), "+v"(fr), "+v"(oo0), "+v"(oo1), "+s"(os), "+s"(fpub));
    static_for<0, 8>([&](auto ic) {
      constexpr int i = ic.value;
      pace<74 + i>(t0);
      t[i] = ld16<NT>(ro, (i < 4 ? xa0 : xa0 ^ 4) * 1024 + c * 16, hb + 32 * (i & 3) * 1024);
    });
    lds_wait<1>(lds + C_O, 4u);
    asm volatile(".p2align 6");
    __builtin_amdgcn_s_sleep(ODELAY);
    v4u o0 = ld16<SC1>(rws, oo0, os), o1 = ld16<SC1>(rws, oo1, os);
    __builtin_amdgcn_sched_barrier(0);
    float cf;
    {
      bool ok = (differs(gm, t12) & 0xfffu) == 0u;
#pragma unroll 1
      for (int it = 0; it < SPIN_CAP && !__all(ok); ++it) {
        if (!ok) {
          gm = ld16<SC1>(rws, c * 128, WS_ML + grp * 16 * 128 + mh * 16);
          ok = (differs(gm, t12) & 0xfffu) == 0u;
        }
        asm volatile("" ::: "memory");
      }
      const float ms = val<12>(gm[0]);
      float M = fmaxf(ms, dpp<XOR1>(ms));
      M = fmaxf(M, dpp<XOR2>(M));
      const float e = __builtin_amdgcn_exp2f((ms - M) * LOG2E);
      float L = e * val<12>(gm[1]);
      L += dpp<XOR1>(L);
      L += dpp<XOR2>(L);
      cf = e * __builtin_amdgcn_rcpf(L);
    }
    asm volatile("" : "+v"(cf));
    __builtin_amdgcn_sched_barrier(0);
    v4u wt[8];
#pragma unroll
    for (int r = 0; r < 4; ++r)
#pragma unroll
      for (int i = 0; i < 4; ++i) {
        u32 lo = pair_lo(t[2 * r], t[2 * r + 1], i), hi = pair_hi(t[2 * r], t[2 * r + 1], i);
        asm volatile("" : "+v"(lo), "+v"(hi));
        wt[2 * i][r] = lo;
        wt[2 * i + 1][r] = hi;
      }
    __builtin_amdgcn_sched_barrier(0);
    settle2(o0, o1, rws, oo0, oo1, os, t12);
    float ov[8];
#pragma unroll
    for (int i = 0; i < 4; ++i) { ov[i] = cf * val<12>(o0[i]); ov[4 + i] = cf * val<12>(o1[i]); }
#define QADD(i, p) "v_add_f32_dpp %" #i ", %" #i ", %" #i " quad_perm:" p " row_mask:0xf bank_mask:0xf\n\t"
#define QADD8(p) QADD(0, p) QADD(1, p) QADD(2, p) QADD(3, p) QADD(4, p) QADD(5, p) QADD(6, p) QADD(7, p)
    asm volatile("s_nop 1\n\t" QADD8("[1,0,3,2]") QADD8("[2,3,0,1]")
                 : "+v"(ov[0]), "+v"(ov[1]), "+v"(ov[2]), "+v"(ov[3]), "+v"(ov[4]), "+v"(ov[5]), "+v"(ov[6]), "+v"(ov[7]));
    const v4u oa = {pkbf(ov[0], ov[1]), pkbf(ov[2], ov[3]), pkbf(ov[4], ov[5]), pkbf(ov[6], ov[7])};
    float f[8];
#pragma unroll
    for (int j = 0; j < 8; ++j) f[j] = mfma(oa, wt[j], v4f{0, 0, 0, 0})[0];
    *(LAS v4f*)(lds + fw) = v4f{f[0], f[1], f[2], f[3]};
    *(LAS v4f*)(lds + fw + PLANE) = v4f{f[4], f[5], f[6], f[7]};
    wg_barrier();
    if (w < 2) {
      const v4f s = (*(LAS v4f*)(lds + fr) + *(LAS v4f*)(lds + fr + PART)) + (*(LAS v4f*)(lds + fr + 2 * PART) + *(LAS v4f*)(lds + fr + 3 * PART));
      __builtin_amdgcn_raw_buffer_store_b128(tag12(s, vmask, t12), rws, lane * 16, fpub, SC1);
    }
  }
}

extern "C" void kernel_launch(void* const* d_in, const int* in_sizes, int n_in, void* d_out, int out_size, void* d_ws, size_t ws_size,
                              hipStream_t stream) {
  Args a;
  a.first[0] = d_in[3]; a.first[1] = d_in[1]; a.second[0] = d_in[2]; a.second[1] = d_in[4];
  a.first[2] = d_in[8]; a.first[3] = d_in[6]; a.second[2] = d_in[7]; a.second[3] = d_in[9];
  a.x = d_in[0];
  a.gw = d_in[10];
  a.out = d_out;
  a.ws = d_ws;
  gated_mix_decode_kernel<<<256, 512, 0, stream>>>(a);
}
```

```cpp
#include <hip/hip_runtime.h>
#include <hip/hip_bf16.h>

typedef unsigned u32;
typedef unsigned long long u64;
typedef u32 v4u __attribute__((ext_vector_type(4)));
typedef u32 v2u __attribute__((ext_vector_type(2)));
typedef float v4f __attribute__((ext_vector_type(4)));
typedef float v2f __attribute__((ext_vector_type(2)));
typedef u64 v2p __attribute__((ext_vector_type(2)));
typedef u64 v4p __attribute__((ext_vector_type(4)));
typedef __bf16 v8b __attribute__((ext_vector_type(8)));
typedef __bf16 v2b __attribute__((ext_vector_type(2)));
#define LAS __attribute__((address_space(3)))

constexpr int TAU = 258;
constexpr int TOFF = -132;
constexpr int GOFF = 14;
constexpr int GWAIT = 300;
constexpr int WIN = 16;
constexpr int PV0 = 21, PV1 = 25, PV2 = 31;
constexpr int ODELAY = 6, FDELAY = 20;
constexpr int CODE_BYTES = 13592;

constexpr int NT = 2, SC1 = 16;
constexpr u32 WS_EPOCH = 0;
constexpr u32 WS_T0 = 256;
constexpr u32 WS_QX = 4096;
constexpr u32 WS_OX = WS_QX + 256 * 2048;
constexpr u32 WS_ML = WS_OX + 256 * 2048;
constexpr u32 WS_FX = WS_ML + 256 * 128;
constexpr u32 WS_END = WS_FX + 256 * 2048;
constexpr int QB = 8;
constexpr int SPIN_CAP = 1 << 15;
constexpr float LOG2E = 1.4426950408889634f, SCALE = 0.08838834764831845f;

constexpr int C_QP = 0, C_QS = 4, C_WO = 8, C_P = 12, C_O = 16, L_TB = 32;
constexpr int L_MLS = 64, L_QS = 256, L_P = L_QS + 1024, L_QP = L_P + 4096;
constexpr int PLANE = 1024 + 128, PART = 2 * PLANE;
constexpr int L_OP = L_QP + 4 * 2048, L_FP = L_OP + 4 * PART, L_END = L_FP + 4 * PART;

struct Args {
  const void* p[8];
  const void *x, *gw;
  void *out, *ws;
};

__device__ __forceinline__ __amdgpu_buffer_rsrc_t mkrsrc(const void* p, u32 bytes) {
  return __builtin_amdgcn_make_buffer_rsrc((void*)p, 0, bytes, 0x00020000);
}
template <int AUX> __device__ __forceinline__ v4u ld16(__amdgpu_buffer_rsrc_t r, int voff, int soff) {
  return __builtin_amdgcn_raw_buffer_load_b128(r, voff, soff, AUX);
}
template <int N> __device__ __forceinline__ void wait_vm() { asm volatile("s_waitcnt vmcnt(%0)" ::"n"(N) : "memory"); }
__device__ __forceinline__ void wg_barrier() {
  __builtin_amdgcn_sched_barrier(0);
  asm volatile("s_waitcnt lgkmcnt(0)\n\ts_barrier" ::: "memory");
  __builtin_amdgcn_sched_barrier(0);
}
template <int CTRL> __device__ __forceinline__ float dpp(float x) {
  return __builtin_bit_cast(float, __builtin_amdgcn_mov_dpp(__builtin_bit_cast(int, x), CTRL, 0xf, 0xf, true));
}
constexpr int XOR1 = 0xB1, XOR2 = 0x4E, XOR7 = 0x141, XOR8 = 0x128;
__device__ __forceinline__ float x16_sum(float x) {
  auto s = __builtin_amdgcn_permlane16_swap(__float_as_uint(x), __float_as_uint(x), false, false);
  return __uint_as_float(s[0]) + __uint_as_float(s[1]);
}
__device__ __forceinline__ float x32_sum(float x) {
  auto s = __builtin_amdgcn_permlane32_swap(__float_as_uint(x), __float_as_uint(x), false, false);
  return __uint_as_float(s[0]) + __uint_as_float(s[1]);
}
__device__ __forceinline__ float x16_max(float x) {
  auto s = __builtin_amdgcn_permlane16_swap(__float_as_uint(x), __float_as_uint(x), false, false);
  return fmaxf(__uint_as_float(s[0]), __uint_as_float(s[1]));
}
__device__ __forceinline__ float x32_max(float x) {
  auto s = __builtin_amdgcn_permlane32_swap(__float_as_uint(x), __float_as_uint(x), false, false);
  return fmaxf(__uint_as_float(s[0]), __uint_as_float(s[1]));
}
__device__ __forceinline__ u32 pkbf(float lo, float hi) {
  v2b r = {(__bf16)lo, (__bf16)hi};
  return __builtin_bit_cast(u32, r);
}
__device__ __forceinline__ float bflo(u32 p) { return __uint_as_float(p << 16); }
__device__ __forceinline__ float bfhi(u32 p) { return __uint_as_float(p & 0xffff0000u); }
__device__ __forceinline__ float rbf(float x) { return bflo(pkbf(x, x)); }
__device__ __forceinline__ v4f mfma(v4u a, v4u b, v4f c) {
  return __builtin_amdgcn_mfma_f32_16x16x32_bf16(__builtin_bit_cast(v8b, a), __builtin_bit_cast(v8b, b), c, 0, 0, 0);
}
__device__ __forceinline__ u32 pair_lo(v4u a, v4u b, int i) { return __builtin_amdgcn_perm(b[i], a[i], 0x05040100u); }
__device__ __forceinline__ u32 pair_hi(v4u a, v4u b, int i) { return __builtin_amdgcn_perm(b[i], a[i], 0x07060302u); }
__device__ __forceinline__ v4u tr_lo(const v4u* t, int i) { return v4u{pair_lo(t[0], t[1], i), pair_lo(t[2], t[3], i), pair_lo(t[4], t[5], i), pair_lo(t[6], t[7], i)}; }
__device__ __forceinline__ v4u tr_hi(const v4u* t, int i) { return v4u{pair_hi(t[0], t[1], i), pair_hi(t[2], t[3], i), pair_hi(t[4], t[5], i), pair_hi(t[6], t[7], i)}; }

template <int TB> __device__ __forceinline__ float val(u32 d) { return __uint_as_float(d & ~((1u << TB) - 1u)); }
__device__ __forceinline__ u32 tagq(float v, u32 t) { return ((__float_as_uint(v) + (1u << (QB - 1))) & ~((1u << QB) - 1u)) | t; }
__device__ __forceinline__ v4u tag12(v4f s, u32 vmask, u32 t) {
  return v4u{(__float_as_uint(s[0]) & vmask) | t, (__float_as_uint(s[1]) & vmask) | t, (__float_as_uint(s[2]) & vmask) | t, (__float_as_uint(s[3]) & vmask) | t};
}
__device__ __forceinline__ u32 differs(v4u g, u32 t) {
  return __builtin_amdgcn_bitop3_b32(g[0], g[1], t, 0x7E) | __builtin_amdgcn_bitop3_b32(g[2], g[3], t, 0x7E);
}
__device__ __forceinline__ void settle2(v4u& a, v4u& b, __amdgpu_buffer_rsrc_t r, int voff0, int voff1, int soff, u32 t12) {
  u32 n;
  asm volatile(
      "s_movk_i32 %2, 0x7fff\n"
      "1:\n\t"
      "v_bitop3_b32 v184, v176, v177, %6 bitop3:0x7e\n\t"
      "v_bitop3_b32 v185, v178, v179, %6 bitop3:0x7e\n\t"
      "v_bitop3_b32 v186, v180, v181, %6 bitop3:0x7e\n\t"
      "v_bitop3_b32 v187, v182, v183, %6 bitop3:0x7e\n\t"
      "v_or3_b32 v184, v184, v185, v186\n\t"
      "v_bitop3_b32 v184, v184, %7, v187 bitop3:0xc8\n\t"
      "v_cmp_ne_u32 vcc, 0, v184\n\t"
      "s_cbranch_vccz 2f\n\t"
      "s_and_b64 exec, exec, vcc\n\t"
      "s_sub_u32 %2, %2, 1\n\t"
      "s_cbranch_scc1 2f\n\t"
      "buffer_load_dwordx4 v[176:179], %3, %4, %5 offen sc1\n\t"
      "buffer_load_dwordx4 v[180:183], %8, %4, %5 offen sc1\n\t"
      "s_waitcnt vmcnt(0)\n\t"
      "s_branch 1b\n"
      "2:\n\t"
      "s_mov_b64 exec, -1"
      : "+{v[176:179]}"(a), "+{v[180:183]}"(b), "=&s"(n)
      : "v"(voff0), "s"(r), "s"(soff), "s"(t12), "s"(0xfffu), "v"(voff1)
      : "v184", "v185", "v186", "v187", "vcc", "scc", "memory");
}

template <int OFF> __device__ __forceinline__ void lds_arrive(u32 zero, u32 one) {
  asm volatile("s_mov_b64 exec, 1\n\tds_add_u32 %0, %1 offset:%2\n\ts_mov_b64 exec, -1" ::"v"(zero), "v"(one), "n"(OFF) : "memory");
}
template <int SLEEP> __device__ __forceinline__ void lds_wait(LAS unsigned char* p, u32 n) {
  while (__hip_atomic_load((LAS u32*)p, __ATOMIC_RELAXED, __HIP_MEMORY_SCOPE_WORKGROUP) < n) {
    if (SLEEP) __builtin_amdgcn_s_sleep(SLEEP);
  }
  asm volatile("" ::: "memory");
}
template <int OFF, int STRIDE> __device__ __forceinline__ v4f lds_wait_sum4(LAS unsigned char* lds, int o) {
  v4f a, b, c, d;
  u32 n;
  do {
    n = __hip_atomic_load((LAS u32*)(lds + OFF), __ATOMIC_RELAXED, __HIP_MEMORY_SCOPE_WORKGROUP);
    asm volatile("" ::: "memory");
    a = *(LAS v4f*)(lds + o); b = *(LAS v4f*)(lds + o + STRIDE); c = *(LAS v4f*)(lds + o + 2 * STRIDE); d = *(LAS v4f*)(lds + o + 3 * STRIDE);
    asm volatile("" : "+v"(a), "+v"(b), "+v"(c), "+v"(d)::"memory");
  } while (__builtin_amdgcn_ballot_w64(n < 4u) != 0ull);
  return (a + b) + (c + d);
}

template <int TICKS> __device__ __forceinline__ void hold(u32 t0) {
  u32 d;
  asm volatile("1:\n\ts_memrealtime vcc\n\ts_waitcnt lgkmcnt(0)\n\ts_sub_u32 %0, vcc_lo, %1\n\ts_cmpk_lt_i32 %0, %2\n\ts_cbranch_scc1 1b"
               : "=&s"(d) : "s"(t0), "n"(TICKS) : "memory", "scc", "vcc");
}
template <int STEP> __device__ __forceinline__ void pace(u32 t0) { hold<TOFF + STEP * TAU / 16>(t0); }

template <int I> struct IC { static constexpr int value = I; };
template <int B, int E, class F> __device__ __forceinline__ void static_for(F&& f) {
  if constexpr (B < E) { f(IC<B>{}); static_for<B + 1, E>(f); }
}

__global__ void __launch_bounds__(512, 2) gated_mix_decode_kernel(Args a) {
  __shared__ __attribute__((aligned(16))) unsigned char lds_raw[L_END];
  LAS unsigned char* lds = (LAS unsigned char*)lds_raw;
  const int bid = blockIdx.x, lane = threadIdx.x & 63, wave = __builtin_amdgcn_readfirstlane(threadIdx.x >> 6);
  u64 pc;
  v2p pa2;
  v4p pb4;
  asm volatile("s_getpc_b64 %2\n\ts_load_dwordx4 %0, %3, %4\n\ts_load_dwordx8 %1, %3, 0x40"
               : "=&s"(pa2), "=&s"(pb4), "=&s"(pc) : "s"(__builtin_amdgcn_kernarg_segment_ptr()), "s"((bid & 4) * 8 + (wave >> 2) * 16));
  __builtin_amdgcn_sched_barrier(0);
  const int grp = (bid & 7) * 2 + (bid >> 7), m = (bid >> 3) & 15, wg = grp * 16 + m;
  const int kvh = grp & 7, mh = m >> 2, ml = m & 3;
  const int w = wave & 3, g = lane >> 4, c = lane & 15;
  const bool vrole = wave < 4;
  const int kvbase = ((mh * 8 + kvh) * 8192 + 6144 + ml * 512) * 256, rot = (bid * 37 + w) & 127;
  const int koff = ((lane >> 2) & 3) * 256 + g * 64 + (lane & 3) * 16;
  const u32 pfc = __builtin_amdgcn_raw_buffer_load_b32(mkrsrc((const void*)(pc & ~255ull), vrole ? CODE_BYTES : 0), c * 64 + g * 65536, (ml * 4 + w) * 1024, 0);
  int vo = vrole ? g * 8 * 8192 + c * 16 : koff, n0 = vrole ? 512 * 8192 : 4 * 8 * 8192 * 256, nx = vrole ? 4096 : 0;
  const int sb = vrole ? (mh * 128 + 32 * w) * 8192 + kvh * 1024 + ml * 256 : kvbase, sr = vrole ? 0 : rot * 1024, st = vrole ? 8192 : 4096;
  int xo = ((lane >> 2) & 3) * 1024 + g * 16, xs = (mh * 128 + 32 * w) * 2, so[8];
#pragma unroll
  for (int i = 0; i < 8; ++i) so[i] = sb + ((sr + i * st) & 0x1ffff);
  u64 t64;
  asm volatile("s_waitcnt lgkmcnt(0)\n\ts_memrealtime %0"
               : "=&s"(t64), "+s"(pa2), "+s"(pb4), "+v"(vo), "+v"(xo), "+s"(n0), "+s"(nx), "+s"(xs), "+s"(so[0]), "+s"(so[1]), "+s"(so[2]),
                 "+s"(so[3]), "+s"(so[4]), "+s"(so[5]), "+s"(so[6]), "+s"(so[7]));
  const void *p0 = (const void*)pa2[0], *p1 = (const void*)pa2[1], *px = (const void*)pb4[0], *pg = (const void*)pb4[1];
  const void *po = (const void*)pb4[2], *pw = (const void*)pb4[3];
  const __amdgpu_buffer_rsrc_t rws = mkrsrc(pw, WS_END);
  const v4u xa = ld16<0>(mkrsrc(px, nx), xo, xs);
  v4u t[32];
  {
    const __amdgpu_buffer_rsrc_t r0 = mkrsrc(p0, n0);
#pragma unroll
    for (int i = 0; i < 8; ++i) t[i] = ld16<NT>(r0, vo, so[i]);
  }
  __builtin_amdgcn_sched_barrier(0);
  u32 ep;
  asm volatile("s_load_dword %0, %1, 0x0" : "=&s"(ep) : "s"(pw) : "memory");
  if (threadIdx.x < 8) *(LAS u32*)(lds + threadIdx.x * 4) = 0u;
  u32 vzero = 0u, vone = 1u, vmask = ~0xfffu;
  asm volatile("" : "+v"(vzero), "+v"(vone), "+v"(vmask));
  wg_barrier();
  asm volatile("" : "+s"(ep), "+s"(t64));
  u32 t0 = (u32)t64;
  const u32 tag = ep + 1u, t12 = tag & 0xfffu;

  if (wave < 4) {
    const __amdgpu_buffer_rsrc_t rv = mkrsrc(p1, 4 * 8 * 8192 * 256);
    const int fb = (bid & 7) >> 1, fc = (bid & 1) * 32 + (bid >> 3);
    const u32 ng = w == 2 ? 1024u : 0u;
    const v4u xv = ld16<0>(mkrsrc((const char*)px + fb * 1024, ng), lane * 16, 0), gv = ld16<0>(mkrsrc(pg, ng), lane * 16, 0);
    __builtin_amdgcn_sched_barrier(0);
    {
      float qp[8];
#pragma unroll
      for (int i = 0; i < 4; ++i) {
        qp[2 * i] = mfma(xa, tr_lo(t, i), v4f{0, 0, 0, 0})[0];
        qp[2 * i + 1] = mfma(xa, tr_hi(t, i), v4f{0, 0, 0, 0})[0];
      }
      *(LAS v4f*)(lds + L_QP + w * 2048 + lane * 32) = v4f{qp[0], qp[1], qp[2], qp[3]};
      *(LAS v4f*)(lds + L_QP + w * 2048 + lane * 32 + 16) = v4f{qp[4], qp[5], qp[6], qp[7]};
      lds_arrive<C_QP>(vzero, vone);
      if (w < 2) {
        lds_wait<0>(lds + C_QP, 4u);
        const int o = L_QP + w * 1024 + lane * 16;
        const v4f s = (*(LAS v4f*)(lds + o) + *(LAS v4f*)(lds + o + 2048)) + (*(LAS v4f*)(lds + o + 4096) + *(LAS v4f*)(lds + o + 6144));
        const u32 tq = tag & ((1u << QB) - 1u);
        __builtin_amdgcn_raw_buffer_store_b128(v4u{tagq(s[0], tq), tagq(s[1], tq), tagq(s[2], tq), tagq(s[3], tq)}, rws, lane * 16, WS_QX + wg * 2048 + w * 1024, SC1);
      }
    }
    float gate = 0.f;
    if (w == 2) {
      float z = 0.f;
#pragma unroll
      for (int i = 0; i < 4; ++i) { const u32 p = pkbf(bflo(xv[i]) * bflo(gv[i]), bfhi(xv[i]) * bfhi(gv[i])); z += bflo(p) + bfhi(p); }
      z += dpp<XOR1>(z); z += dpp<XOR2>(z); z += dpp<XOR7>(z); z += dpp<XOR8>(z);
      z = rbf(x32_sum(x16_sum(z)));
      gate = rbf(1.f / (1.f + __expf(-z)));
      asm volatile("" : "+v"(gate));
    }
    u32 pf0 = 0, pf1 = 0, pf2 = 0;
    if (w == 3) {
      hold<GWAIT>(t0);
      const v2u e = __builtin_amdgcn_raw_buffer_load_b64(rws, (lane & 7) * 8, WS_T0, SC1);
      pf0 = __builtin_amdgcn_raw_buffer_load_b32(rws, (lane & 31) * 64 + (lane >> 5) * (WS_FX - WS_OX), WS_OX + wg * 2048, 0);
      pf1 = __builtin_amdgcn_raw_buffer_load_b32(rws, 0, WS_ML + wg * 128, 0);
      pf2 = __builtin_amdgcn_raw_buffer_load_b32(mkrsrc(po, 4096), 0, (fb * 512 + fc * 8) * 2, 0);
      int dt = e[1] == tag ? (int)(e[0] - t0) : 0;
      dt = dt < -300 ? -300 : dt > 300 ? 300 : dt;
      dt += __builtin_amdgcn_mov_dpp(dt, XOR1, 0xf, 0xf, true);
      dt += __builtin_amdgcn_mov_dpp(dt, XOR2, 0xf, 0xf, true);
      dt += __builtin_amdgcn_mov_dpp(dt, XOR7, 0xf, 0xf, true);
      *(LAS u32*)(lds + L_TB) = t0 + (u32)((dt >> 3) + GOFF);
    }
    wg_barrier();
    t0 = __builtin_amdgcn_readfirstlane(*(LAS u32*)(lds + L_TB));
    v4u pa[4];
    v4f acc[8];
    float eo;
    int ow = L_OP + w * PART + g * 576 + (c & 3) * 144 + (c >> 2) * 4, orr = L_OP + (2 * w + (lane >> 5)) * 576 + ((lane >> 3) & 3) * 144 + (lane & 7) * 16;
    int src = ((lane >> 2) * 16 + (lane & 3) * 4) * 2048;
    int opub = __builtin_amdgcn_readfirstlane(WS_OX + wg * 2048 + w * 1024);
    int fsrc = __builtin_amdgcn_readfirstlane(WS_FX + (fc >> 4) * 2048 + fb * 512 + (fc & 15) * 32), odst = __builtin_amdgcn_readfirstlane((fb * 512 + fc * 8) * 2);
    float gate1 = rbf(1.f - gate);
    asm volatile("" : "+s"(opub), "+s"(fsrc), "+s"(odst), "+v"(gate1));
    const __amdgpu_buffer_rsrc_t rout = mkrsrc(po, 4096);
    static_for<0, 32>([&](auto ic) {
      constexpr int i = ic.value;
      if (i >= WIN) wait_vm<WIN - 1>();
      pace<42 + i>(t0);
      t[i] = ld16<NT>(rv, lane * 16, kvbase + ((4 * i + rot) & 127) * 1024);
      if (i == 31) __hip_atomic_store((LAS u32*)(lds + C_WO), 1u, __ATOMIC_RELAXED, __HIP_MEMORY_SCOPE_WORKGROUP);
      if (i == PV0 || i == PV1 || i == PV2) {
        constexpr int b = i == PV0 ? 0 : i == PV1 ? 1 : 2;
        __builtin_amdgcn_sched_barrier(0);
        if (b == 0) {
          lds_wait<0>(lds + C_P, 4u);
          float mw[4];
#pragma unroll
          for (int j = 0; j < 4; ++j) mw[j] = *(LAS float*)(lds + L_MLS + ((w + j) & 3) * 32 + g * 8);
#pragma unroll
          for (int j = 0; j < 4; ++j) pa[j] = *(LAS v4u*)(lds + L_P + w * 1024 + j * 256 + (lane & ~3) * 4);
          eo = __builtin_amdgcn_exp2f((mw[0] - fmaxf(fmaxf(mw[0], mw[1]), fmaxf(mw[2], mw[3]))) * LOG2E);
          asm volatile("" : "+v"(eo), "+v"(pa[1]), "+v"(pa[2]), "+v"(pa[3]), "+v"(ow), "+v"(orr), "+v"(src));
        }
#pragma unroll
        for (int j = 0; j < 4; ++j) {
          acc[2 * j] = mfma(pa[b], tr_lo(t + 8 * b, j), b ? acc[2 * j] : v4f{0, 0, 0, 0});
          acc[2 * j + 1] = mfma(pa[b], tr_hi(t + 8 * b, j), b ? acc[2 * j + 1] : v4f{0, 0, 0, 0});
        }
        __builtin_amdgcn_sched_barrier(0);
      }
    });
    asm volatile("" ::"v"(pf0), "v"(pf1), "v"(pf2), "v"(pfc));
    {
      v4u vt[8];
#pragma unroll
      for (int r = 0; r < 4; ++r) {
#pragma unroll
        for (int i = 0; i < 4; ++i) {
          vt[2 * i][r] = pair_lo(t[24 + 2 * r], t[25 + 2 * r], i);
          vt[2 * i + 1][r] = pair_hi(t[24 + 2 * r], t[25 + 2 * r], i);
        }
        if (r < 3) __builtin_amdgcn_sched_barrier(0);
      }
#pragma unroll
      for (int j = 0; j < 8; ++j) acc[j] = mfma(pa[3], vt[j], acc[j]);
    }
#pragma unroll
    for (int j = 0; j < 8; ++j) *(LAS float*)(lds + ow + j * 16) = acc[j][0] * eo;
    lds_arrive<C_O>(vzero, vone);
    if (w < 2) __builtin_amdgcn_raw_buffer_store_b128(tag12(lds_wait_sum4<C_O, PART>(lds, orr), vmask, t12), rws, lane * 16, opub, SC1);
    wg_barrier();
    if (w == 2) {
      asm volatile(".p2align 6");
      __builtin_amdgcn_s_sleep(FDELAY);
      v4u f0 = ld16<SC1>(rws, src, fsrc), f1 = ld16<SC1>(rws, src + 16, fsrc);
      settle2(f0, f1, rws, src, src + 16, fsrc, t12);
      float y[4];
#pragma unroll
      for (int i = 0; i < 4; ++i) {
        const float lo = val<12>(f0[i]), hi = val<12>(f1[i]), a0 = lo + dpp<XOR8>(lo), a1 = hi + dpp<XOR8>(hi);
        y[i] = (lane & 8) ? a1 : a0;
      }
#pragma unroll
      for (int i = 0; i < 2; ++i) {
        const float a0 = y[i] + dpp<XOR7>(y[i]), a1 = y[i + 2] + dpp<XOR7>(y[i + 2]);
        y[i] = (lane & 4) ? a1 : a0;
      }
      const float a0 = y[0] + dpp<XOR2>(y[0]), a1 = y[1] + dpp<XOR2>(y[1]);
      float s = (lane & 2) ? a1 : a0;
      s += dpp<XOR1>(s);
      s = x16_sum(s);
      const auto ab = __builtin_amdgcn_permlane32_swap(__float_as_uint(s), __float_as_uint(s), false, false);
      const u32 p = pkbf(__uint_as_float(ab[0]), __uint_as_float(ab[1]));
      const u32 r = pkbf(gate * bflo(p), gate1 * bfhi(p));
      const u32 y16 = pkbf(bflo(r) + bfhi(r), 0.f);
      if (lane < 16 && !(lane & 1)) __builtin_amdgcn_raw_buffer_store_b16((unsigned short)y16, rout, lane & 14, odst, SC1);
    }
  } else {
    const __amdgpu_buffer_rsrc_t rk = mkrsrc(p0, 4 * 8 * 8192 * 256), ro = mkrsrc(p1, 4096 * 1024);
    if (bid < 8 && w == 0 && lane == 0) __builtin_amdgcn_raw_buffer_store_b64(v2u{t0, tag}, rws, 0, WS_T0 + bid * 8, SC1);
    static_for<8, 32>([&](auto ic) {
      constexpr int i = ic.value;
      if (i >= WIN) wait_vm<WIN - 1>();
      pace<8 + i>(t0);
      t[i] = ld16<NT>(rk, koff, kvbase + ((4 * i + rot) & 127) * 1024);
    });
    const int qoff = (lane >> 5) * 8 * 2048 + (lane & 31) * 16, qsrc = WS_QX + (grp * 16 + w) * 2048 + mh * 512;
    v4u gq0 = ld16<SC1>(rws, qoff, qsrc), gq1 = ld16<SC1>(rws, qoff, qsrc + 4 * 2048);
    wg_barrier();
    t0 = __builtin_amdgcn_readfirstlane(*(LAS u32*)(lds + L_TB));
    {
      bool ok = ((differs(gq0, tag) | differs(gq1, tag)) & ((1u << QB) - 1u)) == 0u;
#pragma unroll 1
      for (int it = 0; it < SPIN_CAP && !__all(ok); ++it) {
        if (!ok) {
          gq0 = ld16<SC1>(rws, qoff, qsrc);
          gq1 = ld16<SC1>(rws, qoff, qsrc + 4 * 2048);
          ok = ((differs(gq0, tag) | differs(gq1, tag)) & ((1u << QB) - 1u)) == 0u;
        }
        asm volatile("" ::: "memory");
      }
      float qv[4];
#pragma unroll
      for (int i = 0; i < 4; ++i) qv[i] = x32_sum(val<QB>(gq0[i]) + val<QB>(gq1[i]));
      if (lane < 32) *(LAS v2u*)(lds + L_QS + w * 256 + lane * 8) = v2u{pkbf(qv[0], qv[1]), pkbf(qv[2], qv[3])};
      lds_arrive<C_QS>(vzero, vone);
      lds_wait<0>(lds + C_QS, 4u);
    }
    const v4u qb = *(LAS v4u*)(lds + L_QS + koff);
    const int q = lane & 3;
    float sc[8];
#pragma unroll
    for (int b = 0; b < 4; ++b) {
      float raw[2];
#pragma unroll
      for (int u = 0; u < 2; ++u) {
        const v4f d0 = mfma(t[8 * b + u], qb, v4f{0, 0, 0, 0}), d1 = mfma(t[8 * b + 2 + u], qb, v4f{0, 0, 0, 0});
        const v4f d2 = mfma(t[8 * b + 4 + u], qb, v4f{0, 0, 0, 0}), d3 = mfma(t[8 * b + 6 + u], qb, v4f{0, 0, 0, 0});
        const float r0 = dpp<0xFF>(d0[3]) + (dpp<0xAA>(d0[2]) + (dpp<0x55>(d0[1]) + d0[0]));
        const float r1 = dpp<0xFF>(d1[3]) + (dpp<0xAA>(d1[2]) + (dpp<0x00>(d1[0]) + d1[1]));
        const float r2 = dpp<0xFF>(d2[3]) + (dpp<0x55>(d2[1]) + (dpp<0x00>(d2[0]) + d2[2]));
        const float r3 = dpp<0xAA>(d3[2]) + (dpp<0x55>(d3[1]) + (dpp<0x00>(d3[0]) + d3[3]));
        const float s0 = (q & 1) ? r1 : r0, s1 = (q & 1) ? r3 : r2;
        raw[u] = (q & 2) ? s1 : s0;
      }
      const u32 p1 = pkbf(raw[0], raw[1]);
      const u32 p2 = pkbf(bflo(p1) * SCALE, bfhi(p1) * SCALE);
      sc[2 * b] = bflo(p2);
      sc[2 * b + 1] = bfhi(p2);
    }
    float mx = fmaxf(fmaxf(fmaxf(sc[0], sc[1]), fmaxf(sc[2], sc[3])), fmaxf(fmaxf(sc[4], sc[5]), fmaxf(sc[6], sc[7])));
    mx = fmaxf(mx, dpp<XOR1>(mx));
    mx = fmaxf(mx, dpp<XOR2>(mx));
    mx = x32_max(x16_max(mx));
    float ls = 0.f;
#pragma unroll
    for (int b = 0; b < 4; ++b) {
      const float e0 = __builtin_amdgcn_exp2f((sc[2 * b] - mx) * LOG2E), e1 = __builtin_amdgcn_exp2f((sc[2 * b + 1] - mx) * LOG2E);
      ls += e0 + e1;
      *(LAS u32*)(lds + L_P + w * 1024 + b * 256 + lane * 4) = pkbf(e0, e1);
    }
    ls += dpp<XOR1>(ls);
    ls += dpp<XOR2>(ls);
    ls = x32_sum(x16_sum(ls));
    if (lane < 16 && q == 0) *(LAS v2f*)(lds + L_MLS + w * 32 + (lane >> 2) * 8) = v2f{mx, ls};
    lds_arrive<C_P>(vzero, vone);
    if (w == 3) {
      lds_wait<1>(lds + C_P, 4u);
      float mw[4], lw[4];
#pragma unroll
      for (int i = 0; i < 4; ++i) { const v2f x = *(LAS v2f*)(lds + L_MLS + i * 32 + g * 8); mw[i] = x[0]; lw[i] = x[1]; }
      const float mg = fmaxf(fmaxf(mw[0], mw[1]), fmaxf(mw[2], mw[3]));
      float lg = 0.f;
#pragma unroll
      for (int i = 0; i < 4; ++i) lg += __builtin_amdgcn_exp2f((mw[i] - mg) * LOG2E) * lw[i];
      const u32 um = __float_as_uint(mg) | t12, ul = (__float_as_uint(lg) & vmask) | t12;
      if (c == 0) __builtin_amdgcn_raw_buffer_store_b128(v4u{um, ul, um, ul}, rws, g * 16, WS_ML + wg * 128, SC1);
    }
    if (bid == 0 && w == 0) {
      bool ok = false;
#pragma unroll 1
      for (int it = 0; it < SPIN_CAP && !__all(ok); ++it) {
        u32 d = 0;
#pragma unroll
        for (int i = 0; i < 4; ++i) d |= differs(ld16<SC1>(rws, lane * 2048, WS_QX + i * 64 * 2048), tag);
        ok = (d & ((1u << QB) - 1u)) == 0u;
        asm volatile("" ::: "memory");
      }
      if (lane == 0) __builtin_amdgcn_raw_buffer_store_b32(tag, rws, 0, WS_EPOCH, SC1);
    }
    lds_wait<1>(lds + C_WO, 1u);
    const int xa0 = 8 * w + g + 4 * (mh & 1);
    v4u gm = ld16<SC1>(rws, c * 128, WS_ML + grp * 16 * 128 + mh * 16);
    const int hb = (kvh * 4 + mh) * 128 * 1024 + ml * 256;
    int fw = L_FP + w * PART + lane * 16, fr = L_FP + (lane & 1) * PLANE + (w * 32 + (lane >> 1)) * 16, oo0 = c * 2048 + xa0 * 16, oo1 = oo0 ^ 64;
    int os = WS_OX + grp * 16 * 2048 + mh * 512, fpub = WS_FX + wg * 2048 + w * 1024;
    asm volatile("" : "+v"(fw), "+v"(fr), "+v"(oo0), "+v"(oo1), "+s"(os), "+s"(fpub));
    static_for<0, 8>([&](auto ic) {
      constexpr int i = ic.value;
      pace<74 + i>(t0);
      t[i] = ld16<NT>(ro, (i < 4 ? xa0 : xa0 ^ 4) * 1024 + c * 16, hb + 32 * (i & 3) * 1024);
    });
    lds_wait<1>(lds + C_O, 4u);
    asm volatile(".p2align 6");
    __builtin_amdgcn_s_sleep(ODELAY);
    v4u o0 = ld16<SC1>(rws, oo0, os), o1 = ld16<SC1>(rws, oo1, os);
    __builtin_amdgcn_sched_barrier(0);
    float cf;
    {
      bool ok = (differs(gm, t12) & 0xfffu) == 0u;
#pragma unroll 1
      for (int it = 0; it < SPIN_CAP && !__all(ok); ++it) {
        if (!ok) {
          gm = ld16<SC1>(rws, c * 128, WS_ML + grp * 16 * 128 + mh * 16);
          ok = (differs(gm, t12) & 0xfffu) == 0u;
        }
        asm volatile("" ::: "memory");
      }
      const float ms = val<12>(gm[0]);
      float M = fmaxf(ms, dpp<XOR1>(ms));
      M = fmaxf(M, dpp<XOR2>(M));
      const float e = __builtin_amdgcn_exp2f((ms - M) * LOG2E);
      float L = e * val<12>(gm[1]);
      L += dpp<XOR1>(L);
      L += dpp<XOR2>(L);
      cf = e * __builtin_amdgcn_rcpf(L);
    }
    asm volatile("" : "+v"(cf));
    __builtin_amdgcn_sched_barrier(0);
    v4u wt[8];
#pragma unroll
    for (int r = 0; r < 4; ++r)
#pragma unroll
      for (int i = 0; i < 4; ++i) {
        u32 lo = pair_lo(t[2 * r], t[2 * r + 1], i), hi = pair_hi(t[2 * r], t[2 * r + 1], i);
        asm volatile("" : "+v"(lo), "+v"(hi));
        wt[2 * i][r] = lo;
        wt[2 * i + 1][r] = hi;
      }
    __builtin_amdgcn_sched_barrier(0);
    settle2(o0, o1, rws, oo0, oo1, os, t12);
    float ov[8];
#pragma unroll
    for (int i = 0; i < 4; ++i) { ov[i] = cf * val<12>(o0[i]); ov[4 + i] = cf * val<12>(o1[i]); }
#define QADD(i, p) "v_add_f32_dpp %" #i ", %" #i ", %" #i " quad_perm:" p " row_mask:0xf bank_mask:0xf\n\t"
#define QADD8(p) QADD(0, p) QADD(1, p) QADD(2, p) QADD(3, p) QADD(4, p) QADD(5, p) QADD(6, p) QADD(7, p)
    asm volatile("s_nop 1\n\t" QADD8("[1,0,3,2]") QADD8("[2,3,0,1]")
                 : "+v"(ov[0]), "+v"(ov[1]), "+v"(ov[2]), "+v"(ov[3]), "+v"(ov[4]), "+v"(ov[5]), "+v"(ov[6]), "+v"(ov[7]));
    const v4u oa = {pkbf(ov[0], ov[1]), pkbf(ov[2], ov[3]), pkbf(ov[4], ov[5]), pkbf(ov[6], ov[7])};
    float f[8];
#pragma unroll
    for (int j = 0; j < 8; ++j) f[j] = mfma(oa, wt[j], v4f{0, 0, 0, 0})[0];
    *(LAS v4f*)(lds + fw) = v4f{f[0], f[1], f[2], f[3]};
    *(LAS v4f*)(lds + fw + PLANE) = v4f{f[4], f[5], f[6], f[7]};
    wg_barrier();
    if (w < 2) {
      const v4f s = (*(LAS v4f*)(lds + fr) + *(LAS v4f*)(lds + fr + PART)) + (*(LAS v4f*)(lds + fr + 2 * PART) + *(LAS v4f*)(lds + fr + 3 * PART));
      __builtin_amdgcn_raw_buffer_store_b128(tag12(s, vmask, t12), rws, lane * 16, fpub, SC1);
    }
  }
}

extern "C" void kernel_launch(void* const* d_in, const int* in_sizes, int n_in, void* d_out, int out_size, void* d_ws, size_t ws_size,
                              hipStream_t stream) {
  Args a;
  a.p[0] = d_in[3]; a.p[1] = d_in[2]; a.p[2] = d_in[1]; a.p[3] = d_in[4];
  a.p[4] = d_in[8]; a.p[5] = d_in[7]; a.p[6] = d_in[6]; a.p[7] = d_in[9];
  a.x = d_in[0];
  a.gw = d_in[10];
  a.out = d_out;
  a.ws = d_ws;
  gated_mix_decode_kernel<<<256, 512, 0, stream>>>(a);
}
```
